```python
import torch

def _softmax(x, axis=-1):
    return torch.softmax(x, dim=axis)
def _gelu(x):
    return torch.nn.functional.gelu(x, approximate="tanh")
def _silu(x):
    return torch.nn.functional.silu(x)
def _sigmoid(x):
    return torch.sigmoid(x)
def _layernorm(x, axis=-1, eps=1e-5):
    m = x.mean(dim=axis, keepdim=True)
    v = x.var(dim=axis, keepdim=True, unbiased=False)
    return (x - m) / torch.sqrt(v + eps)
def _scatter_add(tgt, idx, src, axis=0):
    return tgt.clone().index_add_(axis, idx, src)
def _segment_sum(src, seg, n):
    out = torch.zeros((n,) + src.shape[1:], dtype=src.dtype)
    return out.index_add_(0, seg, src)
def _pad(x, pw):
    flat = tuple(v for pair in reversed(pw) for v in pair)
    return torch.nn.functional.pad(x, flat)

kernel_name = 'gated_mix__softmax_full_mha__linear_kernel_hedgehog__prompt'

def setup_inputs():

    _w = (lambda a: (lambda t: t.to(torch.bfloat16) if t.is_floating_point() else t)(torch.as_tensor(a)))
    def _A_setup():
        B, S, D = 1, 2048, 512
        return {
            "x": _w((lambda *s: torch.randn(*s, dtype=torch.bfloat16) * 0.05)(B, S, D)),
            "wqkv": _w((lambda *s: torch.randn(*s, dtype=torch.bfloat16) * 0.8)(D, 3 * D)),
            "wo": _w((lambda *s: torch.randn(*s, dtype=torch.bfloat16) * 0.05)(D, D)),
        }
    _A_in = _A_setup()

    def _B_setup():
        B, S, D, H = 1, 2048, 512, 32
        Dh = D // H
        return {
            "x": _w((lambda *s: torch.randn(*s, dtype=torch.bfloat16) * 0.05)(B, S, D)),
            "wqkv": _w((lambda *s: torch.randn(*s, dtype=torch.bfloat16) * 1.6)(D, 3 * D)),
            "w_phi": _w((lambda *s: torch.randn(*s, dtype=torch.bfloat16) * 1.6)(H, Dh, Dh) * (Dh ** -0.5)),
            "wo": _w((lambda *s: torch.randn(*s, dtype=torch.bfloat16) * 0.05)(D, D)),
        }
    _B_in = _B_setup()

    return {
        "A_x": _A_in["x"],
        "A_wqkv": _A_in["wqkv"],
        "A_wo": _A_in["wo"],
        "B_x": _B_in["x"],
        "B_wqkv": _B_in["wqkv"],
        "B_w_phi": _B_in["w_phi"],
        "B_wo": _B_in["wo"],
        "gate_w": _w((lambda *s: torch.randn(*s, dtype=torch.bfloat16) * 0.05)(512) * 16.0),
    }

def reference(A_x, A_wqkv, A_wo, B_x, B_wqkv, B_w_phi, B_wo, gate_w):
    def _A_ref(x, wqkv, wo):
        B, S, D = x.shape
        H = 32; Dh = D // H
        qkv = (lambda x, *d: x.permute(*d))((x @ wqkv).reshape(B, S, 3, H, Dh), 2, 0, 3, 1, 4)
        q, k, v = qkv[0], qkv[1], qkv[2]
        scale = Dh ** -0.5
        scores = torch.einsum("bhqd,bhkd->bhqk", q, k) * scale
        p = _softmax(scores, axis=-1)
        o = torch.einsum("bhqk,bhkd->bhqd", p, v)
        return (lambda x, *d: x.permute(*d))(o, 0, 2, 1, 3).reshape(B, S, D) @ wo

    def _B_ref(x, wqkv, w_phi, wo):
        B, S, D = x.shape
        H = 32; Dh = D // H
        qkv = (lambda x, *d: x.permute(*d))((x @ wqkv).reshape(B, S, 3, H, Dh), 2, 0, 3, 1, 4)
        q, k, v = qkv[0], qkv[1], qkv[2]
        pq = torch.einsum("bhsd,hdf->bhsf", q, w_phi)
        pk = torch.einsum("bhsd,hdf->bhsf", k, w_phi)
        phi_q = torch.concatenate([_softmax(pq, axis=-1), _softmax(-pq, axis=-1)], axis=-1)
        phi_k = torch.concatenate([_softmax(pk, axis=-1), _softmax(-pk, axis=-1)], axis=-1)
        kv = torch.einsum("bhsf,bhsd->bhfd", phi_k, v)
        z  = torch.einsum("bhsf->bhf", phi_k)
        num = torch.einsum("bhqf,bhfd->bhqd", phi_q, kv)
        den = torch.einsum("bhqf,bhf->bhq", phi_q, z)[..., None] + 1e-6
        o = num / den
        return (lambda x, *d: x.permute(*d))(o, 0, 2, 1, 3).reshape(B, S, D) @ wo

    x = A_x
    g = _sigmoid((x * gate_w).sum(-1, keepdims=True))
    a = _A_ref(x, A_wqkv, A_wo)
    b = _B_ref(x, B_wqkv, B_w_phi, B_wo)
    return g * a + (1 - g) * b
```

```cpp
#include <hip/hip_runtime.h>
#include <hip/hip_bf16.h>
#include <stdint.h>

typedef __bf16 bf16x8 __attribute__((ext_vector_type(8)));
typedef __bf16 bf16x2 __attribute__((ext_vector_type(2)));
typedef short s16x4 __attribute__((ext_vector_type(4)));
typedef float f32x16 __attribute__((ext_vector_type(16)));
typedef float f32x4 __attribute__((ext_vector_type(4)));
typedef float f32x2 __attribute__((ext_vector_type(2)));
typedef unsigned u32x4 __attribute__((ext_vector_type(4)));
typedef unsigned u32x2 __attribute__((ext_vector_type(2)));
#define LAS __attribute__((address_space(3)))
typedef LAS char* lds_ptr;

#define MFMA(a, b, c) __builtin_amdgcn_mfma_f32_32x32x16_bf16((a), (b), (c), 0, 0, 0)
#define WAIT_BAR(N) asm volatile("s_waitcnt vmcnt(" #N ") lgkmcnt(0)\n\ts_barrier" ::: "memory")

constexpr float LOG2E = 1.4426950408889634f;

constexpr size_t WS_QF = 0;
constexpr size_t WS_KF = WS_QF + (2u << 20);
constexpr size_t WS_VF = WS_KF + (2u << 20);
constexpr size_t WS_PQ = WS_VF + (2u << 20);
constexpr size_t WS_KV = WS_PQ + (4u << 20);
constexpr size_t WS_OF = WS_KV + (2u << 20);
constexpr size_t WS_G = WS_OF + (4u << 20);

__device__ __forceinline__ void glds16(const void* g, unsigned lds_base) {
    unsigned sv;
    asm volatile("s_mov_b32 %0, m0\n\ts_mov_b32 m0, %2\n\ts_nop 0\n\tglobal_load_lds_dwordx4 %1, off\n\ts_mov_b32 m0, %0"
                 : "=&s"(sv) : "v"(g), "s"(lds_base) : "memory");
}
__device__ __forceinline__ void glds4(const void* g, unsigned lds_base) {
    unsigned sv;
    asm volatile("s_mov_b32 %0, m0\n\ts_mov_b32 m0, %2\n\ts_nop 0\n\tglobal_load_lds_dword %1, off\n\ts_mov_b32 m0, %0"
                 : "=&s"(sv) : "v"(g), "s"(lds_base) : "memory");
}
__device__ __forceinline__ unsigned pk2(float lo, float hi) {
    f32x2 v = {lo, hi};
    return __builtin_bit_cast(unsigned, __builtin_convertvector(v, bf16x2));
}
template <int O>
__device__ __forceinline__ bf16x8 pack8(const f32x16& a) {
    u32x4 r = {pk2(a[O], a[O + 1]), pk2(a[O + 2], a[O + 3]), pk2(a[O + 4], a[O + 5]), pk2(a[O + 6], a[O + 7])};
    return __builtin_bit_cast(bf16x8, r);
}
__device__ __forceinline__ bf16x8 ld_b128(lds_ptr p) { return *(const LAS bf16x8*)p; }
__device__ __forceinline__ u32x2 ld_tr(lds_ptr p) {
    return __builtin_bit_cast(u32x2, __builtin_amdgcn_ds_read_tr16_b64_v4i16((LAS s16x4*)p));
}
__device__ __forceinline__ bf16x8 ld_tr2(lds_ptr p0, lds_ptr p1) {
    u32x2 a = ld_tr(p0), b = ld_tr(p1);
    u32x4 r = {a[0], a[1], b[0], b[1]};
    return __builtin_bit_cast(bf16x8, r);
}
__device__ __forceinline__ float xhalf_max(float m) {
    auto r = __builtin_amdgcn_permlane32_swap(__float_as_uint(m), __float_as_uint(m), false, false);
    return __builtin_fmaxf(__uint_as_float(r[0]), __uint_as_float(r[1]));
}
__device__ __forceinline__ float xhalf_sum(float m) {
    auto r = __builtin_amdgcn_permlane32_swap(__float_as_uint(m), __float_as_uint(m), false, false);
    return __uint_as_float(r[0]) + __uint_as_float(r[1]);
}
#define MX3(a, b, c) __builtin_fmaxf(__builtin_fmaxf((a), (b)), (c))

__device__ __forceinline__ void dual_softmax(f32x16& t) {
#pragma unroll
    for (int g = 0; g < 2; ++g) {
        const int o = 8 * g;
        float m = MX3(t[o], t[o + 1], t[o + 2]);
        m = MX3(m, t[o + 3], t[o + 4]);
        m = MX3(m, t[o + 5], t[o + 6]);
        m = __builtin_fmaxf(m, t[o + 7]);
        m = xhalf_max(m);
        const float nm = -m * LOG2E;
        float s = 0.f;
#pragma unroll
        for (int i = 0; i < 8; ++i) { t[o + i] = __builtin_amdgcn_exp2f(__builtin_fmaf(t[o + i], LOG2E, nm)); s += t[o + i]; }
        s = xhalf_sum(s);
        const float r = __builtin_amdgcn_rcpf(s);
#pragma unroll
        for (int i = 0; i < 8; ++i) t[o + i] *= r;
    }
}

constexpr int K1_STAGE = 40960, K1_LDS = 4 * K1_STAGE;
__global__ __launch_bounds__(512, 2) void k1_qkv(const uint16_t* __restrict__ x, const uint16_t* __restrict__ wA,
                                                 const uint16_t* __restrict__ wB, const uint16_t* __restrict__ wphi,
                                                 const uint16_t* __restrict__ gatew, char* __restrict__ ws) {
    extern __shared__ __attribute__((aligned(16))) char smem[];
    const lds_ptr lds = (lds_ptr)smem;
    const unsigned lds0 = (unsigned)(uintptr_t)lds;
    const int tid = threadIdx.x, lane = tid & 63, r32 = lane & 31, hh = lane >> 5;
    const int wid = __builtin_amdgcn_readfirstlane(tid >> 6);
    const int hq = blockIdx.x & 7, idx = blockIdx.x >> 3, rb = idx >> 1, br = idx & 1;
    const uint16_t* W = br ? wB : wA;

    const int grow = blockIdx.x * 8 + wid;
    const u32x4 gx = *(const u32x4*)(x + (size_t)grow * 512 + lane * 8);
    const u32x4 gw = *(const u32x4*)(gatew + lane * 8);

    const int lr = lane >> 3, lc = lane & 7;
    const uint16_t* xs0 = x + (size_t)(rb * 128 + wid * 16 + lr) * 512 + ((lc ^ ((lr >> 1) & 7)) * 8);
    const uint16_t* xs1 = x + (size_t)(rb * 128 + wid * 16 + 8 + lr) * 512 + ((lc ^ (((8 + lr) >> 1) & 7)) * 8);
    const uint16_t* wsrc = W + (size_t)(wid * 8 + lr) * 1536 + hq * 64 + ((lc ^ (((lr >> 1) & 1) << 2)) * 8);
#define K1_ISSUE(s)                                                                      \
    do {                                                                                 \
        const unsigned b_ = lds0 + ((s)&3) * K1_STAGE;                                   \
        glds16(xs0 + (s)*64, b_ + wid * 2048);                                           \
        glds16(xs1 + (s)*64, b_ + wid * 2048 + 1024);                                    \
        glds16(wsrc + (size_t)(s)*64 * 1536, b_ + 16384 + wid * 1024);                   \
        glds16(wsrc + (size_t)(s)*64 * 1536 + 512, b_ + 16384 + 8192 + wid * 1024);      \
        glds16(wsrc + (size_t)(s)*64 * 1536 + 1024, b_ + 16384 + 16384 + wid * 1024);    \
    } while (0)
    K1_ISSUE(0); K1_ISSUE(1); K1_ISSUE(2);

    const int rt = wid & 3, p = wid >> 2;
    const int xrow = rt * 32 + r32, xsw = (xrow >> 1) & 7;
    lds_ptr xa[4];
#pragma unroll
    for (int ks = 0; ks < 4; ++ks) xa[ks] = lds + xrow * 128 + (((2 * ks + hh) ^ xsw) * 16);
    const int q4 = (lane & 15) >> 2, pp = lane & 3, g1 = (lane >> 4) & 1;
    const int wch = (p * 4 + g1 * 2 + (pp >> 1)) ^ (((q4 >> 1) & 1) << 2);
    const lds_ptr wa = lds + 16384 + (8 * hh + q4) * 128 + wch * 16 + (pp & 1) * 8;

    f32x16 aq = {}, ak = {}, av = {};
#define K1_COMPUTE(s)                                                                         \
    do {                                                                                      \
        _Pragma("unroll") for (int ks = 0; ks < 4; ++ks) {                                    \
            const int bo = ((s)&3) * K1_STAGE;                                                \
            const bf16x8 xf = ld_b128(xa[ks] + bo);                                           \
            const bf16x8 fq = ld_tr2(wa + bo + ks * 2048, wa + bo + ks * 2048 + 512);         \
            const bf16x8 fk = ld_tr2(wa + bo + 8192 + ks * 2048, wa + bo + 8192 + ks * 2048 + 512);   \
            const bf16x8 fv = ld_tr2(wa + bo + 16384 + ks * 2048, wa + bo + 16384 + ks * 2048 + 512); \
            aq = MFMA(fq, xf, aq);                                                            \
            ak = MFMA(fk, xf, ak);                                                            \
            av = MFMA(xf, fv, av);                                                            \
        }                                                                                     \
    } while (0)
    WAIT_BAR(10); K1_ISSUE(3); K1_COMPUTE(0);
    WAIT_BAR(10); K1_ISSUE(4); K1_COMPUTE(1);
    WAIT_BAR(10); K1_ISSUE(5); K1_COMPUTE(2);
    WAIT_BAR(10); K1_ISSUE(6); K1_COMPUTE(3);
    WAIT_BAR(10); K1_ISSUE(7); K1_COMPUTE(4);
    WAIT_BAR(10);
    glds4(wphi + hq * 1024 + wid * 128 + lane * 2, lds0 + wid * 256);
    K1_COMPUTE(5);
    WAIT_BAR(6); K1_COMPUTE(6);
    WAIT_BAR(0); K1_COMPUTE(7);

    {
        float s = 0.f;
#pragma unroll
        for (int i = 0; i < 4; ++i) {
            s = __builtin_fmaf(__uint_as_float(gx[i] << 16), __uint_as_float(gw[i] << 16), s);
            s = __builtin_fmaf(__uint_as_float(gx[i] & 0xffff0000u), __uint_as_float(gw[i] & 0xffff0000u), s);
        }
#pragma unroll
        for (int o = 32; o > 0; o >>= 1) s += __shfl_xor(s, o);
        if (lane == 0) ((float*)(ws + WS_G))[grow] = 1.f / (1.f + __expf(-s));
    }

    const int tile = rb * 4 + rt, h0 = hq * 4 + p * 2;
    if (br == 0) {
        const float c = 0.25f * LOG2E;
#pragma unroll
        for (int i = 0; i < 16; ++i) aq[i] *= c;
        char* qd = ws + WS_QF + ((size_t)h0 * 64 + tile) * 1024 + lane * 16;
        char* kd = ws + WS_KF + ((size_t)h0 * 64 + tile) * 1024 + lane * 16;
        *(bf16x8*)qd = pack8<0>(aq); *(bf16x8*)(qd + 65536) = pack8<8>(aq);
        *(bf16x8*)kd = pack8<0>(ak); *(bf16x8*)(kd + 65536) = pack8<8>(ak);
        char* vd = ws + WS_VF + ((size_t)(h0 + (r32 >> 4)) * 64 + tile) * 1024 + (hh * 16 + (r32 & 15)) * 16;
        *(bf16x8*)vd = pack8<0>(av); *(bf16x8*)(vd + 512) = pack8<8>(av);
    } else {
        const lds_ptr tsc = lds + K1_STAGE + wid * 2048;
        const lds_ptr kvs = lds + K1_STAGE + 16384;
        const bf16x8 v0 = pack8<0>(av), v1 = pack8<8>(av);
        const unsigned sgn = (r32 & 16) ? 0x80008000u : 0u;
        const f32x16 z16 = {};
#pragma unroll
        for (int e = 0; e < 2; ++e) {
            const int h = h0 + e;
            const lds_ptr wp = lds + (p * 2 + e) * 512 + (4 * hh + q4) * 32 + pp * 8;
            u32x4 wu = __builtin_bit_cast(u32x4, ld_tr2(wp, wp + 256));
#pragma unroll
            for (int i = 0; i < 4; ++i) wu[i] ^= sgn;
            const bf16x8 wf = __builtin_bit_cast(bf16x8, wu);
            const bf16x8 qf = e ? pack8<8>(aq) : pack8<0>(aq);
            const bf16x8 kf = e ? pack8<8>(ak) : pack8<0>(ak);
            f32x16 tq = MFMA(wf, qf, z16);
            f32x16 tk = MFMA(wf, kf, z16);
            dual_softmax(tq);
            char* pd = ws + WS_PQ + ((size_t)h * 64 + tile) * 2048 + lane * 16;
            *(bf16x8*)pd = pack8<0>(tq); *(bf16x8*)(pd + 1024) = pack8<8>(tq);
            dual_softmax(tk);
#pragma unroll
            for (int g = 0; g < 4; ++g) {
                u32x2 w2 = {pk2(tk[4 * g], tk[4 * g + 1]), pk2(tk[4 * g + 2], tk[4 * g + 3])};
                *(LAS u32x2*)(tsc + r32 * 64 + (8 * g + 4 * hh) * 2) = w2;
            }
            const lds_ptr tr = tsc + (4 * hh + q4) * 64 + g1 * 32 + pp * 8;
            const bf16x8 a0 = ld_tr2(tr, tr + 512), a1 = ld_tr2(tr + 1024, tr + 1536);
            const bool mine = (r32 >> 4) == e;
            u32x4 b0 = __builtin_bit_cast(u32x4, v0), b1 = __builtin_bit_cast(u32x4, v1);
#pragma unroll
            for (int i = 0; i < 4; ++i) { b0[i] = mine ? b0[i] : 0x3f803f80u; b1[i] = mine ? b1[i] : 0x3f803f80u; }
            f32x16 kv = MFMA(a0, __builtin_bit_cast(bf16x8, b0), z16);
            kv = MFMA(a1, __builtin_bit_cast(bf16x8, b1), kv);
            const lds_ptr kd = kvs + ((p * 4 + rt) * 2 + e) * 4096 + (lane ^ (e << 4)) * 16;
#pragma unroll
            for (int g = 0; g < 4; ++g) *(LAS f32x4*)(kd + g * 1024) = f32x4{kv[4 * g], kv[4 * g + 1], kv[4 * g + 2], kv[4 * g + 3]};
        }
        __syncthreads();
        const int e = rt >> 1;
#pragma unroll
        for (int u = 0; u < 2; ++u) {
            const int g = (rt & 1) * 2 + u;
            f32x4 s = {};
#pragma unroll
            for (int t = 0; t < 4; ++t) s += *(const LAS f32x4*)(kvs + ((p * 4 + t) * 2 + e) * 4096 + g * 1024 + lane * 16);
            *(f32x4*)(ws + WS_KV + ((size_t)(h0 + e) * 16 + rb) * 4096 + g * 1024 + lane * 16) = s;
        }
    }
}

constexpr int K2_V = 65536, K2_VS = 1040, K2_X = K2_V + 64 * K2_VS;
constexpr int K2_KV = K2_X + 8 * 9 * 256, K2_FLAG = K2_KV + 4096, K2_LDS = K2_FLAG + 16;

__device__ __forceinline__ void attn_pass(lds_ptr kp, lds_ptr vp0, lds_ptr vp1, const bf16x8 qf, const f32x16& negm,
                                          f32x16& o, int t0, int t1) {
#pragma unroll 4
    for (int t = t0; t < t1; ++t) {
        const bf16x8 kf = ld_b128(kp + t * 1024);
        const bf16x8 va = ld_b128(vp0 + t * K2_VS), vb = ld_b128(vp1 + t * K2_VS);
        f32x16 s = MFMA(kf, qf, negm);
#pragma unroll
        for (int i = 0; i < 16; ++i) s[i] = __builtin_amdgcn_exp2f(s[i]);
        o = MFMA(va, pack8<0>(s), o);
        o = MFMA(vb, pack8<8>(s), o);
    }
}
__device__ __forceinline__ float tile_max(const f32x16& s) {
    float a = MX3(s[0], s[1], s[2]), b = MX3(s[3], s[4], s[5]);
    a = MX3(a, s[6], s[7]); b = MX3(b, s[8], s[9]);
    a = MX3(a, s[10], s[11]); b = MX3(b, s[12], s[13]);
    a = MX3(a, s[14], s[15]);
    return __builtin_fmaxf(a, b);
}

__global__ __launch_bounds__(1024, 4) void k2_attn(char* __restrict__ ws) {
    extern __shared__ __attribute__((aligned(16))) char smem[];
    const lds_ptr lds = (lds_ptr)smem;
    const unsigned lds0 = (unsigned)(uintptr_t)lds;
    const int tid = threadIdx.x, lane = tid & 63, r32 = lane & 31, hh = lane >> 5;
    const int wid = __builtin_amdgcn_readfirstlane(tid >> 6);
    const int idx = blockIdx.x >> 3, h = (blockIdx.x & 7) * 4 + (idx >> 3), qb = idx & 7;
    const int qt = wid & 7, kh = wid >> 3, tile = qb * 8 + qt;

    const bf16x8 qf = *(const bf16x8*)(ws + WS_QF + ((size_t)h * 64 + tile) * 1024 + lane * 16);
    {
        const int isv = wid & 1, tl = ((wid >> 1) & 3) + (wid >> 3) * 32;
        const char* src = ws + (isv ? WS_VF : WS_KF) + ((size_t)h * 64 + tl) * 1024 + lane * 16;
        const unsigned dst = lds0 + (isv ? K2_V + tl * K2_VS : tl * 1024), dstep = isv ? 4 * K2_VS : 4096;
#pragma unroll
        for (int c = 0; c < 8; ++c) glds16(src + c * 4096, dst + c * dstep);
    }
    if (tid < 64) *(LAS u32x4*)(lds + K2_V + tid * K2_VS + 1024) = u32x4{0x3f803f80u, 0x3f803f80u, 0x3f803f80u, 0x3f803f80u};
    if (tid == 64) *(LAS unsigned*)(lds + K2_FLAG) = 0u;

    const lds_ptr kp = lds + lane * 16;
    const bool ones = r32 >= 16 && (r32 < 20 || r32 >= 28);
    const int vo = (hh * 16 + (r32 < 16 ? r32 : 0)) * 16;
    const lds_ptr vp0 = lds + K2_V + (ones ? 1024 : vo), vp1 = lds + K2_V + (ones ? 1024 : vo + 512);

    WAIT_BAR(7);
    const f32x16 z16 = {};
    float m = xhalf_max(tile_max(MFMA(ld_b128(kp), qf, z16)));
    f32x16 negm, o = {};
#pragma unroll
    for (int i = 0; i < 16; ++i) negm[i] = -m;
    const int tb = kh * 32;
    attn_pass(kp, vp0, vp1, qf, negm, o, tb, tb + 4);
    WAIT_BAR(0);
    attn_pass(kp, vp0, vp1, qf, negm, o, tb + 4, tb + 32);

    {
        const float* kvp = (const float*)(ws + WS_KV + (size_t)h * 16 * 4096) + tid;
        float s = 0.f;
#pragma unroll
        for (int i = 0; i < 16; ++i) s += kvp[i * 1024];
        *(LAS float*)(lds + K2_KV + tid * 4) = s;
    }
    const float g = ((const float*)(ws + WS_G))[tile * 32 + r32];
    const lds_ptr xp = lds + K2_X + qt * 9 * 256 + lane * 4;
    char* od = ws + WS_OF + ((size_t)tile * 64 + kh * 32 + h) * 1024 + lane * 16;

    for (int attempt = 0;; ++attempt) {
        float l = hh ? o[12] : o[8];
        if (kh) {
#pragma unroll
            for (int i = 0; i < 8; ++i) *(LAS float*)(xp + i * 256) = o[i];
            *(LAS float*)(xp + 8 * 256) = l;
        }
        __syncthreads();
        if (!kh) {
#pragma unroll
            for (int i = 0; i < 8; ++i) o[i] += *(const LAS float*)(xp + i * 256);
            l += *(const LAS float*)(xp + 8 * 256);
            float amax = MX3(__builtin_fabsf(o[0]), __builtin_fabsf(o[1]), __builtin_fabsf(o[2]));
            amax = MX3(amax, __builtin_fabsf(o[3]), __builtin_fabsf(o[4]));
            amax = MX3(amax, __builtin_fabsf(o[5]), __builtin_fabsf(o[6]));
            amax = __builtin_fmaxf(amax, __builtin_fabsf(o[7]));
            const float chk = (o[0] + o[1] + o[2] + o[3] + o[4] + o[5] + o[6] + o[7]) * 0.f;
            const bool bad = !(l <= 1.0995116e12f) || !(amax <= 3.0e38f) || !(chk == 0.f);
            if (attempt == 0 && __any(bad)) { if (lane == 0) *(LAS unsigned*)(lds + K2_FLAG) = 1u; }
            const float sc = g / l;
#pragma unroll
            for (int i = 0; i < 8; ++i) o[i] *= sc;
            *(bf16x8*)od = pack8<0>(o);
        } else if (attempt == 0) {
            f32x16 kv;
#pragma unroll
            for (int gq = 0; gq < 4; ++gq) {
                const f32x4 t = *(const LAS f32x4*)(lds + K2_KV + gq * 1024 + lane * 16);
                kv[4 * gq] = t[0]; kv[4 * gq + 1] = t[1]; kv[4 * gq + 2] = t[2]; kv[4 * gq + 3] = t[3];
            }
            const char* pq = ws + WS_PQ + ((size_t)h * 64 + tile) * 2048 + lane * 16;
            f32x16 nb = MFMA(pack8<0>(kv), *(const bf16x8*)pq, z16);
            nb = MFMA(pack8<8>(kv), *(const bf16x8*)(pq + 1024), nb);
            const float sc = (1.f - g) / (nb[8] + 1e-6f);
#pragma unroll
            for (int i = 0; i < 8; ++i) nb[i] *= sc;
            *(bf16x8*)od = pack8<0>(nb);
        }
        if (attempt) break;
        __syncthreads();
        if (*(const LAS unsigned*)(lds + K2_FLAG) == 0u) break;
        float mm = -INFINITY;
        for (int t = 0; t < 64; ++t) mm = __builtin_fmaxf(mm, tile_max(MFMA(ld_b128(kp + t * 1024), qf, z16)));
        mm = xhalf_max(mm);
#pragma unroll
        for (int i = 0; i < 16; ++i) { negm[i] = -mm; o[i] = 0.f; }
        attn_pass(kp, vp0, vp1, qf, negm, o, tb, tb + 32);
    }
}

constexpr int K3_STAGE = 32768, K3_LDS = 5 * K3_STAGE;
__global__ __launch_bounds__(512, 2) void k3_out(const char* __restrict__ ws, const uint16_t* __restrict__ woA,
                                                 const uint16_t* __restrict__ woB, uint16_t* __restrict__ out) {
    extern __shared__ __attribute__((aligned(16))) char smem[];
    const lds_ptr lds = (lds_ptr)smem;
    const unsigned lds0 = (unsigned)(uintptr_t)lds;
    const int tid = threadIdx.x, lane = tid & 63, hh = lane >> 5;
    const int wid = __builtin_amdgcn_readfirstlane(tid >> 6);
    const int idx = blockIdx.x >> 3, rb = (blockIdx.x & 7) * 4 + (idx >> 3), cb = idx & 7;
    const int br = wid >> 2, w4 = wid & 3;
    const int lr = lane >> 3, lc = lane & 7;
    const char* osrc = ws + WS_OF + ((size_t)(rb * 2) * 64 + br * 32 + w4) * 1024 + lane * 16;
    const uint16_t* wsrc = (br ? woB : woA) + (size_t)(w4 * 16 + lr) * 512 + cb * 64 + ((lc ^ (((lr >> 1) & 1) << 2)) * 8);
#define K3_ISSUE(j)                                                         \
    do {                                                                    \
        const unsigned b_ = lds0 + ((j) % 5) * K3_STAGE + wid * 4096;       \
        glds16(osrc + (j)*4096, b_);                                        \
        glds16(osrc + (j)*4096 + 65536, b_ + 1024);                         \
        glds16(wsrc + (size_t)(j)*64 * 512, b_ + 2048);                     \
        glds16(wsrc + (size_t)(j)*64 * 512 + 8 * 512, b_ + 3072);           \
    } while (0)
    K3_ISSUE(0); K3_ISSUE(1); K3_ISSUE(2); K3_ISSUE(3);
    const int rt = w4 & 1, ct = w4 >> 1;
    const int q4 = (lane & 15) >> 2, pp = lane & 3, g1 = (lane >> 4) & 1;
    const lds_ptr oa = lds + br * 16384 + rt * 1024 + lane * 16;
    const int wch = (ct * 4 + g1 * 2 + (pp >> 1)) ^ (((q4 >> 1) & 1) << 2);
    const lds_ptr wa = lds + br * 16384 + 2048 + (4 * hh + q4) * 128 + wch * 16 + (pp & 1) * 8;
    f32x16 acc = {};
#define K3_COMPUTE(j)                                                              \
    do {                                                                           \
        _Pragma("unroll") for (int ks = 0; ks < 4; ++ks) {                         \
            const int bo = ((j) % 5) * K3_STAGE + ks * 4096;                       \
            acc = MFMA(ld_tr2(wa + bo, wa + bo + 1024), ld_b128(oa + bo), acc);    \
        }                                                                          \
    } while (0)
    WAIT_BAR(12); K3_ISSUE(4); K3_COMPUTE(0);
    WAIT_BAR(12); K3_ISSUE(5); K3_COMPUTE(1);
    WAIT_BAR(12); K3_ISSUE(6); K3_COMPUTE(2);
    WAIT_BAR(12); K3_ISSUE(7); K3_COMPUTE(3);
    WAIT_BAR(12); K3_COMPUTE(4);
    WAIT_BAR(8); K3_COMPUTE(5);
    WAIT_BAR(4); K3_COMPUTE(6);
    WAIT_BAR(0); K3_COMPUTE(7);
    const lds_ptr red = lds + 3 * K3_STAGE;
    {
        const lds_ptr d = red + br * (64 * 272) + (rt * 32 + (lane & 31)) * 272 + (ct * 32 + 4 * hh) * 4;
#pragma unroll
        for (int g = 0; g < 4; ++g) *(LAS f32x4*)(d + g * 32) = f32x4{acc[4 * g], acc[4 * g + 1], acc[4 * g + 2], acc[4 * g + 3]};
    }
    __syncthreads();
    {
        const int row = tid >> 3, c8 = tid & 7;
        const lds_ptr s = red + row * 272 + c8 * 32;
        const f32x4 a0 = *(const LAS f32x4*)s + *(const LAS f32x4*)(s + 64 * 272);
        const f32x4 a1 = *(const LAS f32x4*)(s + 16) + *(const LAS f32x4*)(s + 64 * 272 + 16);
        u32x4 r = {pk2(a0[0], a0[1]), pk2(a0[2], a0[3]), pk2(a1[0], a1[1]), pk2(a1[2], a1[3])};
        *(u32x4*)(out + (size_t)(rb * 64 + row) * 512 + cb * 64 + c8 * 8) = r;
    }
}

extern "C" void kernel_launch(void* const* d_in, const int* in_sizes, int n_in, void* d_out, int out_size, void* d_ws,
                              size_t ws_size, hipStream_t stream) {
    const uint16_t* x = (const uint16_t*)d_in[0];
    const uint16_t* wA = (const uint16_t*)d_in[1];
    const uint16_t* woA = (const uint16_t*)d_in[2];
    const uint16_t* wB = (const uint16_t*)d_in[4];
    const uint16_t* wphi = (const uint16_t*)d_in[5];
    const uint16_t* woB = (const uint16_t*)d_in[6];
    const uint16_t* gw = (const uint16_t*)d_in[7];
    char* ws = (char*)d_ws;
    (void)hipFuncSetAttribute((const void*)k1_qkv, hipFuncAttributeMaxDynamicSharedMemorySize, K1_LDS);
    (void)hipFuncSetAttribute((const void*)k2_attn, hipFuncAttributeMaxDynamicSharedMemorySize, K2_LDS);
    (void)hipFuncSetAttribute((const void*)k3_out, hipFuncAttributeMaxDynamicSharedMemorySize, K3_LDS);
    k1_qkv<<<256, 512, K1_LDS, stream>>>(x, wA, wB, wphi, gw, ws);
    k2_attn<<<256, 1024, K2_LDS, stream>>>(ws);
    k3_out<<<256, 512, K3_LDS, stream>>>(ws, woA, woB, (uint16_t*)d_out);
}
```

```cpp
#include <hip/hip_runtime.h>
#include <hip/hip_bf16.h>
#include <stdint.h>

typedef __bf16 bf16x8 __attribute__((ext_vector_type(8)));
typedef __bf16 bf16x2 __attribute__((ext_vector_type(2)));
typedef short s16x4 __attribute__((ext_vector_type(4)));
typedef float f32x16 __attribute__((ext_vector_type(16)));
typedef float f32x4 __attribute__((ext_vector_type(4)));
typedef float f32x2 __attribute__((ext_vector_type(2)));
typedef unsigned u32x4 __attribute__((ext_vector_type(4)));
typedef unsigned u32x2 __attribute__((ext_vector_type(2)));
typedef unsigned long long u64;
#define LAS __attribute__((address_space(3)))
#define GAS __attribute__((address_space(1)))
typedef LAS char* lds_ptr;
typedef GAS u64 gu64;
#define RLX_AGENT __ATOMIC_RELAXED, __HIP_MEMORY_SCOPE_AGENT

#define MFMA(a, b, c) __builtin_amdgcn_mfma_f32_32x32x16_bf16((a), (b), (c), 0, 0, 0)
#define WAIT_BAR(N) asm volatile("s_waitcnt vmcnt(" #N ") lgkmcnt(0)\n\ts_barrier" ::: "memory")
#define LDS_BAR() asm volatile("s_waitcnt lgkmcnt(0)\n\ts_barrier" ::: "memory")
#define SBAR() __builtin_amdgcn_sched_barrier(0)

#define STAMP_DECL
#define STAMP(n)
#define STAMP_OUT(kid, cnt)

constexpr float LOG2E = 1.4426950408889634f;

constexpr unsigned WS_E = 0;
constexpr unsigned WS_F1 = 4096;
constexpr unsigned WS_F2 = 8192;
constexpr unsigned WS_F3 = 12288;
constexpr unsigned WS_F4 = 16384;
constexpr unsigned WS_G = 65536;
constexpr unsigned WS_QF = 131072;
constexpr unsigned WS_KF = WS_QF + (2u << 20);
constexpr unsigned WS_VF = WS_KF + (2u << 20);
constexpr unsigned WS_PQ = WS_VF + (2u << 20);
constexpr unsigned WS_OF = WS_PQ + (4u << 20);
constexpr unsigned KVP = 2176;
constexpr unsigned WS_KV = WS_OF + (4u << 20);
constexpr unsigned WS_WO = WS_KV + 32 * 32 * KVP;
constexpr unsigned WS_END = WS_WO + (1u << 20);

constexpr int P1_STAGE = 40960;
constexpr int VS = 1040, L_V = 64 * VS;
constexpr int L_X = L_V + 64 * VS;
constexpr int L_CNT = L_X, L_GW = L_X + 256;
constexpr int L_EX = L_X + 256;
constexpr int L_TR = L_EX + 8 * 9 * 256;
constexpr int LDS_BYTES = 163840;
static_assert(L_X >= 3 * P1_STAGE && L_TR + 4 * KVP <= LDS_BYTES, "lds map");
enum { C_F1 = 0, C_F2, C_F3, C_F4, C_KV0, C_KV1, C_KV2, C_G0, C_EX, C_S0, C_S1, C_P1, C_P2, C_P4 };

__device__ __forceinline__ void glds16(const void* g, unsigned lds_base) {
    unsigned sv;
    asm volatile("s_mov_b32 %0, m0\n\ts_mov_b32 m0, %2\n\ts_nop 0\n\tglobal_load_lds_dwordx4 %1, off\n\ts_mov_b32 m0, %0"
                 : "=&s"(sv) : "v"(g), "s"(lds_base) : "memory");
}
__device__ __forceinline__ void glds4(const void* g, unsigned lds_base) {
    unsigned sv;
    asm volatile("s_mov_b32 %0, m0\n\ts_mov_b32 m0, %2\n\ts_nop 0\n\tglobal_load_lds_dword %1, off\n\ts_mov_b32 m0, %0"
                 : "=&s"(sv) : "v"(g), "s"(lds_base) : "memory");
}
__device__ __forceinline__ unsigned pk2(float lo, float hi) {
    f32x2 v = {lo, hi};
    return __builtin_bit_cast(unsigned, __builtin_convertvector(v, bf16x2));
}
__device__ __forceinline__ float dot2(unsigned a, unsigned b, float c) {
    return __builtin_amdgcn_fdot2_f32_bf16(__builtin_bit_cast(bf16x2, a), __builtin_bit_cast(bf16x2, b), c, false);
}
template <int O>
__device__ __forceinline__ u32x4 pack8u(const f32x16& a) {
    return u32x4{pk2(a[O], a[O + 1]), pk2(a[O + 2], a[O + 3]), pk2(a[O + 4], a[O + 5]), pk2(a[O + 6], a[O + 7])};
}
template <int O>
__device__ __forceinline__ bf16x8 pack8(const f32x16& a) { return __builtin_bit_cast(bf16x8, pack8u<O>(a)); }
__device__ __forceinline__ bf16x8 ld_b128(lds_ptr p) { return *(const LAS bf16x8*)p; }
__device__ __forceinline__ u32x2 ld_tr(lds_ptr p) {
    return __builtin_bit_cast(u32x2, __builtin_amdgcn_ds_read_tr16_b64_v4i16((LAS s16x4*)p));
}
__device__ __forceinline__ bf16x8 ld_tr2(lds_ptr p0, lds_ptr p1) {
    u32x2 a = ld_tr(p0), b = ld_tr(p1);
    u32x4 r = {a[0], a[1], b[0], b[1]};
    return __builtin_bit_cast(bf16x8, r);
}
__device__ __forceinline__ float xhalf_max(float m) {
    auto r = __builtin_amdgcn_permlane32_swap(__float_as_uint(m), __float_as_uint(m), false, false);
    return __builtin_fmaxf(__uint_as_float(r[0]), __uint_as_float(r[1]));
}
__device__ __forceinline__ float xhalf_sum(float m) {
    auto r = __builtin_amdgcn_permlane32_swap(__float_as_uint(m), __float_as_uint(m), false, false);
    return __uint_as_float(r[0]) + __uint_as_float(r[1]);
}
#define MX3(a, b, c) __builtin_fmaxf(__builtin_fmaxf((a), (b)), (c))
typedef __amdgpu_buffer_rsrc_t rsrc_t;
__device__ __forceinline__ void st_wt(rsrc_t r, unsigned off, u32x4 v) { __builtin_amdgcn_raw_buffer_store_b128(v, r, off, 0, 16); }
__device__ __forceinline__ u32x4 ld_dev(rsrc_t r, unsigned off, unsigned soff) { return __builtin_amdgcn_raw_buffer_load_b128(r, off, soff, 16); }

__device__ __forceinline__ u64 uniform64(u64 v) {
    return ((u64)(unsigned)__builtin_amdgcn_readfirstlane((unsigned)(v >> 32)) << 32) | (unsigned)__builtin_amdgcn_readfirstlane((unsigned)v);
}
__device__ __forceinline__ void signal(lds_ptr cnt, unsigned n, gu64* flag, u64 token, int lane) {
    asm volatile("s_waitcnt vmcnt(0)" ::: "memory");
    if (lane == 0) {
        const unsigned old = __hip_atomic_fetch_add((LAS unsigned*)cnt, 1u, __ATOMIC_RELAXED, __HIP_MEMORY_SCOPE_WORKGROUP);
        if (old == n - 1) __hip_atomic_store(flag, token, RLX_AGENT);
    }
}
__device__ __forceinline__ void lds_arrive(lds_ptr cnt, int lane) {
    asm volatile("s_waitcnt lgkmcnt(0)" ::: "memory");
    if (lane == 0) __hip_atomic_fetch_add((LAS unsigned*)cnt, 1u, __ATOMIC_RELAXED, __HIP_MEMORY_SCOPE_WORKGROUP);
}
template <int SLEEP = 1>
__device__ __forceinline__ void lds_wait(lds_ptr cnt, unsigned n) {
#pragma unroll 1
    for (unsigned spins = 0; spins < (1u << 24); ++spins) {
        if ((unsigned)__builtin_amdgcn_readfirstlane(__hip_atomic_load((LAS unsigned*)cnt, __ATOMIC_RELAXED, __HIP_MEMORY_SCOPE_WORKGROUP)) >= n) break;
        __builtin_amdgcn_s_sleep(SLEEP);
    }
    asm volatile("" ::: "memory");
}
template <int SLEEP>
__device__ __forceinline__ void wait32(gu64* flags, u64 token, int lane) {
#pragma unroll 1
    for (unsigned spins = 0; spins < (1u << 20); ++spins) {
        const u64 v = __hip_atomic_load(flags + (lane & 31), RLX_AGENT);
        if (__all(v == token)) break;
        __builtin_amdgcn_s_sleep(SLEEP);
    }
    __builtin_amdgcn_fence(__ATOMIC_ACQUIRE, "wavefront");
    asm volatile("" ::: "memory");
}

__device__ __forceinline__ void dual_softmax2(f32x16& t, f32x16& u) {
    float m[4];
#pragma unroll
    for (int g = 0; g < 4; ++g) {
        const f32x16& a = g & 2 ? u : t; const int o = 8 * (g & 1);
        m[g] = MX3(MX3(a[o], a[o + 1], a[o + 2]), MX3(a[o + 3], a[o + 4], a[o + 5]), __builtin_fmaxf(a[o + 6], a[o + 7]));
    }
#pragma unroll
    for (int g = 0; g < 4; ++g) m[g] = -LOG2E * xhalf_max(m[g]);
#pragma unroll
    for (int i = 0; i < 8; ++i) {
        t[i] = __builtin_amdgcn_exp2f(__builtin_fmaf(t[i], LOG2E, m[0])); t[8 + i] = __builtin_amdgcn_exp2f(__builtin_fmaf(t[8 + i], LOG2E, m[1]));
        u[i] = __builtin_amdgcn_exp2f(__builtin_fmaf(u[i], LOG2E, m[2])); u[8 + i] = __builtin_amdgcn_exp2f(__builtin_fmaf(u[8 + i], LOG2E, m[3]));
    }
#pragma unroll
    for (int g = 0; g < 4; ++g) {
        const f32x16& a = g & 2 ? u : t; const int o = 8 * (g & 1);
        m[g] = ((a[o] + a[o + 1]) + (a[o + 2] + a[o + 3])) + ((a[o + 4] + a[o + 5]) + (a[o + 6] + a[o + 7]));
    }
#pragma unroll
    for (int g = 0; g < 4; ++g) m[g] = __builtin_amdgcn_rcpf(xhalf_sum(m[g]));
#pragma unroll
    for (int i = 0; i < 8; ++i) { t[i] *= m[0]; t[8 + i] *= m[1]; u[i] *= m[2]; u[8 + i] *= m[3]; }
}
__device__ __forceinline__ float tile_max(const f32x16& s) {
    float a = MX3(s[0], s[1], s[2]), b = MX3(s[3], s[4], s[5]);
    a = MX3(a, s[6], s[7]); b = MX3(b, s[8], s[9]);
    a = MX3(a, s[10], s[11]); b = MX3(b, s[12], s[13]);
    a = MX3(a, s[14], s[15]);
    return __builtin_fmaxf(a, b);
}

#define ATT_IT0 \
    "v_exp_f32 v83, v83\n\t" \
    "v_exp_f32 v84, v84\n\t" \
    "v_cvt_pk_bf16_f32 v76, v76, v77\n\t" \
    "v_cvt_pk_bf16_f32 v77, v78, v79\n\t" \
    "v_cvt_pk_bf16_f32 v78, v80, v81\n\t" \
    "v_cvt_pk_bf16_f32 v79, v82, v83\n\t" \
    "v_exp_f32 v85, v85\n\t" \
    "v_exp_f32 v86, v86\n\t" \
    "v_mfma_f32_32x32x16_bf16 v[108:123], v[148:151], v[76:79], v[108:123]\n\t" \
    "ds_read_b128 v[156:159], v165 offset:%c[vo]\n\t" \
    "v_exp_f32 v87, v87\n\t" \
    "v_exp_f32 v88, v88\n\t" \
    "v_exp_f32 v89, v89\n\t" \
    "v_exp_f32 v90, v90\n\t" \
    "v_exp_f32 v91, v91\n\t" \
    "v_exp_f32 v92, v92\n\t" \
    "v_cvt_pk_bf16_f32 v84, v84, v85\n\t" \
    "v_cvt_pk_bf16_f32 v85, v86, v87\n\t" \
    "v_cvt_pk_bf16_f32 v86, v88, v89\n\t" \
    "v_cvt_pk_bf16_f32 v87, v90, v91\n\t" \
    "v_exp_f32 v93, v93\n\t" \
    "v_exp_f32 v94, v94\n\t" \
    "v_mfma_f32_32x32x16_bf16 v[108:123], v[152:155], v[84:87], v[108:123]\n\t" \
    "ds_read_b128 v[160:163], v166 offset:%c[vo]\n\t" \
    "v_exp_f32 v95, v95\n\t" \
    "v_exp_f32 v96, v96\n\t" \
    "v_exp_f32 v97, v97\n\t" \
    "v_exp_f32 v98, v98\n\t" \
    "v_mfma_f32_32x32x16_bf16 v[76:91], v[144:147], v[36:39], v[44:59]\n\t" \
    "ds_read_b128 v[140:143], v164 offset:%c[ko]\n\t" \
    "v_exp_f32 v99, v99\n\t" \
    "v_exp_f32 v100, v100\n\t" \
    "v_cvt_pk_bf16_f32 v92, v92, v93\n\t" \
    "v_cvt_pk_bf16_f32 v93, v94, v95\n\t" \
    "v_cvt_pk_bf16_f32 v94, v96, v97\n\t" \
    "v_cvt_pk_bf16_f32 v95, v98, v99\n\t" \
    "v_exp_f32 v101, v101\n\t" \
    "v_exp_f32 v102, v102\n\t" \
    "v_mfma_f32_32x32x16_bf16 v[124:139], v[148:151], v[92:95], v[124:139]\n\t" \
    "v_exp_f32 v103, v103\n\t" \
    "v_exp_f32 v104, v104\n\t" \
    "v_exp_f32 v105, v105\n\t" \
    "v_exp_f32 v106, v106\n\t" \
    "v_exp_f32 v107, v107\n\t" \
    "v_exp_f32 v76, v76\n\t" \
    "v_cvt_pk_bf16_f32 v100, v100, v101\n\t" \
    "v_cvt_pk_bf16_f32 v101, v102, v103\n\t" \
    "v_cvt_pk_bf16_f32 v102, v104, v105\n\t" \
    "v_cvt_pk_bf16_f32 v103, v106, v107\n\t" \
    "v_exp_f32 v77, v77\n\t" \
    "v_exp_f32 v78, v78\n\t" \
    "v_mfma_f32_32x32x16_bf16 v[124:139], v[152:155], v[100:103], v[124:139]\n\t" \
    "v_exp_f32 v79, v79\n\t" \
    "v_exp_f32 v80, v80\n\t" \
    "v_exp_f32 v81, v81\n\t" \
    "v_exp_f32 v82, v82\n\t" \
    "v_mfma_f32_32x32x16_bf16 v[92:107], v[144:147], v[40:43], v[60:75]\n\t" \
    "s_waitcnt lgkmcnt(0)\n\t"
#define ATT_IT1 \
    "v_exp_f32 v83, v83\n\t" \
    "v_exp_f32 v84, v84\n\t" \
    "v_cvt_pk_bf16_f32 v76, v76, v77\n\t" \
    "v_cvt_pk_bf16_f32 v77, v78, v79\n\t" \
    "v_cvt_pk_bf16_f32 v78, v80, v81\n\t" \
    "v_cvt_pk_bf16_f32 v79, v82, v83\n\t" \
    "v_exp_f32 v85, v85\n\t" \
    "v_exp_f32 v86, v86\n\t" \
    "v_mfma_f32_32x32x16_bf16 v[108:123], v[156:159], v[76:79], v[108:123]\n\t" \
    "ds_read_b128 v[148:151], v165 offset:%c[vo]\n\t" \
    "v_exp_f32 v87, v87\n\t" \
    "v_exp_f32 v88, v88\n\t" \
    "v_exp_f32 v89, v89\n\t" \
    "v_exp_f32 v90, v90\n\t" \
    "v_exp_f32 v91, v91\n\t" \
    "v_exp_f32 v92, v92\n\t" \
    "v_cvt_pk_bf16_f32 v84, v84, v85\n\t" \
    "v_cvt_pk_bf16_f32 v85, v86, v87\n\t" \
    "v_cvt_pk_bf16_f32 v86, v88, v89\n\t" \
    "v_cvt_pk_bf16_f32 v87, v90, v91\n\t" \
    "v_exp_f32 v93, v93\n\t" \
    "v_exp_f32 v94, v94\n\t" \
    "v_mfma_f32_32x32x16_bf16 v[108:123], v[160:163], v[84:87], v[108:123]\n\t" \
    "ds_read_b128 v[152:155], v166 offset:%c[vo]\n\t" \
    "v_exp_f32 v95, v95\n\t" \
    "v_exp_f32 v96, v96\n\t" \
    "v_exp_f32 v97, v97\n\t" \
    "v_exp_f32 v98, v98\n\t" \
    "v_mfma_f32_32x32x16_bf16 v[76:91], v[140:143], v[36:39], v[44:59]\n\t" \
    "ds_read_b128 v[144:147], v164 offset:%c[ko]\n\t" \
    "v_exp_f32 v99, v99\n\t" \
    "v_exp_f32 v100, v100\n\t" \
    "v_cvt_pk_bf16_f32 v92, v92, v93\n\t" \
    "v_cvt_pk_bf16_f32 v93, v94, v95\n\t" \
    "v_cvt_pk_bf16_f32 v94, v96, v97\n\t" \
    "v_cvt_pk_bf16_f32 v95, v98, v99\n\t" \
    "v_exp_f32 v101, v101\n\t" \
    "v_exp_f32 v102, v102\n\t" \
    "v_mfma_f32_32x32x16_bf16 v[124:139], v[156:159], v[92:95], v[124:139]\n\t" \
    "v_exp_f32 v103, v103\n\t" \
    "v_exp_f32 v104, v104\n\t" \
    "v_exp_f32 v105, v105\n\t" \
    "v_exp_f32 v106, v106\n\t" \
    "v_exp_f32 v107, v107\n\t" \
    "v_exp_f32 v76, v76\n\t" \
    "v_cvt_pk_bf16_f32 v100, v100, v101\n\t" \
    "v_cvt_pk_bf16_f32 v101, v102, v103\n\t" \
    "v_cvt_pk_bf16_f32 v102, v104, v105\n\t" \
    "v_cvt_pk_bf16_f32 v103, v106, v107\n\t" \
    "v_exp_f32 v77, v77\n\t" \
    "v_exp_f32 v78, v78\n\t" \
    "v_mfma_f32_32x32x16_bf16 v[124:139], v[160:163], v[100:103], v[124:139]\n\t" \
    "v_exp_f32 v79, v79\n\t" \
    "v_exp_f32 v80, v80\n\t" \
    "v_exp_f32 v81, v81\n\t" \
    "v_exp_f32 v82, v82\n\t" \
    "v_mfma_f32_32x32x16_bf16 v[92:107], v[140:143], v[40:43], v[60:75]\n\t" \
    "s_waitcnt lgkmcnt(0)\n\t"
#define ATT_LAST1 \
    "v_exp_f32 v83, v83\n\t" \
    "v_exp_f32 v84, v84\n\t" \
    "v_cvt_pk_bf16_f32 v76, v76, v77\n\t" \
    "v_cvt_pk_bf16_f32 v77, v78, v79\n\t" \
    "v_cvt_pk_bf16_f32 v78, v80, v81\n\t" \
    "v_cvt_pk_bf16_f32 v79, v82, v83\n\t" \
    "v_exp_f32 v85, v85\n\t" \
    "v_exp_f32 v86, v86\n\t" \
    "v_mfma_f32_32x32x16_bf16 v[108:123], v[156:159], v[76:79], v[108:123]\n\t" \
    "v_exp_f32 v87, v87\n\t" \
    "v_exp_f32 v88, v88\n\t" \
    "v_exp_f32 v89, v89\n\t" \
    "v_exp_f32 v90, v90\n\t" \
    "v_exp_f32 v91, v91\n\t" \
    "v_exp_f32 v92, v92\n\t" \
    "v_cvt_pk_bf16_f32 v84, v84, v85\n\t" \
    "v_cvt_pk_bf16_f32 v85, v86, v87\n\t" \
    "v_cvt_pk_bf16_f32 v86, v88, v89\n\t" \
    "v_cvt_pk_bf16_f32 v87, v90, v91\n\t" \
    "v_exp_f32 v93, v93\n\t" \
    "v_exp_f32 v94, v94\n\t" \
    "v_mfma_f32_32x32x16_bf16 v[108:123], v[160:163], v[84:87], v[108:123]\n\t" \
    "v_exp_f32 v95, v95\n\t" \
    "v_exp_f32 v96, v96\n\t" \
    "v_exp_f32 v97, v97\n\t" \
    "v_exp_f32 v98, v98\n\t" \
    "v_exp_f32 v99, v99\n\t" \
    "v_exp_f32 v100, v100\n\t" \
    "v_cvt_pk_bf16_f32 v92, v92, v93\n\t" \
    "v_cvt_pk_bf16_f32 v93, v94, v95\n\t" \
    "v_cvt_pk_bf16_f32 v94, v96, v97\n\t" \
    "v_cvt_pk_bf16_f32 v95, v98, v99\n\t" \
    "v_exp_f32 v101, v101\n\t" \
    "v_exp_f32 v102, v102\n\t" \
    "v_mfma_f32_32x32x16_bf16 v[124:139], v[156:159], v[92:95], v[124:139]\n\t" \
    "v_exp_f32 v103, v103\n\t" \
    "v_exp_f32 v104, v104\n\t" \
    "v_exp_f32 v105, v105\n\t" \
    "v_exp_f32 v106, v106\n\t" \
    "v_exp_f32 v107, v107\n\t" \
    "s_nop 0\n\t" \
    "v_cvt_pk_bf16_f32 v100, v100, v101\n\t" \
    "v_cvt_pk_bf16_f32 v101, v102, v103\n\t" \
    "v_cvt_pk_bf16_f32 v102, v104, v105\n\t" \
    "v_cvt_pk_bf16_f32 v103, v106, v107\n\t" \
    "s_nop 1\n\t" \
    "v_mfma_f32_32x32x16_bf16 v[124:139], v[160:163], v[100:103], v[124:139]\n\t" \
    "s_nop 15\n\t"
#define ATT_HEAD \
    "s_nop 1\n\t" \
    "v_mfma_f32_32x32x16_bf16 v[76:91], v[140:143], v[36:39], v[44:59]\n\t" \
    "v_mfma_f32_32x32x16_bf16 v[92:107], v[140:143], v[40:43], v[60:75]\n\t" \
    "s_nop 7\n\t" \
    "s_nop 3\n\t" \
    "v_exp_f32 v76, v76\n\t" \
    "v_exp_f32 v77, v77\n\t" \
    "v_exp_f32 v78, v78\n\t" \
    "v_exp_f32 v79, v79\n\t" \
    "v_exp_f32 v80, v80\n\t" \
    "v_exp_f32 v81, v81\n\t" \
    "v_exp_f32 v82, v82\n\t"
#define ATT_OPS(R) "+{v[76:91]}"(R.sa), "+{v[92:107]}"(R.sb), "+{v[108:123]}"(R.oa), "+{v[124:139]}"(R.ob), \
    "+{v[140:143]}"(R.k0), "+{v[144:147]}"(R.k1), "+{v[148:151]}"(R.v00), "+{v[152:155]}"(R.v01), "+{v[156:159]}"(R.v10), "+{v[160:163]}"(R.v11)
#define ATT_INS(R) "{v[36:39]}"(R.qa), "{v[40:43]}"(R.qb), "{v[44:59]}"(R.na), "{v[60:75]}"(R.nb), "{v164}"(R.kp), "{v165}"(R.vp0), "{v166}"(R.vp1)
struct AttRegs { f32x16 sa, sb, oa, ob, na, nb; bf16x8 qa, qb, k0, k1, v00, v01, v10, v11; unsigned kp, vp0, vp1; };
template <int T>
__device__ __forceinline__ void att_iter(AttRegs& R) {
    if constexpr (T == 31) asm volatile(ATT_LAST1 : ATT_OPS(R) : ATT_INS(R) : "memory");
    else if constexpr (T & 1) asm volatile(ATT_IT1 : ATT_OPS(R) : ATT_INS(R), [vo] "i"((T + 1) * VS), [ko] "i"((T + 2) * VS) : "memory");
    else asm volatile(ATT_IT0 : ATT_OPS(R) : ATT_INS(R), [vo] "i"((T + 1) * VS), [ko] "i"((T + 2) * VS) : "memory");
}
template <int T0, int T1>
__device__ __forceinline__ void att_run(AttRegs& R) {
    if constexpr (T0 < T1) { att_iter<T0>(R); att_run<T0 + 1, T1>(R); }
}
#define CNT(i) (lds + L_CNT + (i)*4)

__global__ __launch_bounds__(768, 3) void fused(const uint16_t* __restrict__ x, const uint16_t* __restrict__ wA,
                                                const uint16_t* __restrict__ woA, const uint16_t* __restrict__ wB,
                                                const uint16_t* __restrict__ wphi, const uint16_t* __restrict__ woB,
                                                const uint16_t* __restrict__ gatew, uint16_t* __restrict__ out, char* ws) {
    extern __shared__ __attribute__((aligned(16))) char smem[];
    const lds_ptr lds = (lds_ptr)smem;
    const unsigned lds0 = (unsigned)(uintptr_t)lds;
    int tid = threadIdx.x, lane = tid & 63, r32 = lane & 31, hh = lane >> 5;
    const int wid = __builtin_amdgcn_readfirstlane(tid >> 6);
    const int xc = blockIdx.x & 7, idx = blockIdx.x >> 3;
    const rsrc_t wsr = __builtin_amdgcn_make_buffer_rsrc(ws, 0, WS_END, 0x00020000);
    int q4 = (lane & 15) >> 2, pp = lane & 3, g1 = (lane >> 4) & 1, lr = lane >> 3, lc = lane & 7;
    lds_ptr kp = lds + lane * 16;
#define RELANE() do { asm volatile("" : "+v"(tid)); lane = tid & 63; r32 = lane & 31; hh = lane >> 5; q4 = (lane & 15) >> 2; pp = lane & 3; \
    g1 = (lane >> 4) & 1; lr = lane >> 3; lc = lane & 7; kp = lds + lane * 16; } while (0)
    const f32x16 z16 = {};
    STAMP_DECL; STAMP(0);
    u64 token;

    const int hq = (xc & 3) * 2 + (idx & 1), set = (idx >> 1) & 1, rb = (xc >> 2) * 8 + (idx >> 2);
#define ROTA 4
#define ROTM 4
    const int rot = ((idx >> 2) * ROTA + (idx & 3) * 2) & ROTM;
    const int h = xc * 4 + (idx >> 3), qb = idx & 7;
    const int rb3 = xc * 4 + (idx >> 3), cb = idx & 7;
    gu64* const f1 = (gu64*)(ws + WS_F1) + hq * 32 + rb * 2 + set;
    if (tid < 16) *(LAS unsigned*)(lds + L_CNT + tid * 4) = 0u;

    if (wid < 8) {
        {
            const u64 epoch = __hip_atomic_load((gu64*)(ws + WS_E), RLX_AGENT);
            const unsigned vx0 = (wid * 16 + lr) * 1024 + ((lc ^ (lr >> 1)) * 16), vx1 = (wid * 16 + 8 + lr) * 1024 + ((lc ^ (4 + (lr >> 1))) * 16);
            const unsigned vw = (wid * 8 + lr) * 3072 + ((lc ^ (((lr >> 1) & 1) << 2)) * 16);
            const uint16_t* xb = x + (size_t)rb * 128 * 512;
            const uint16_t* w0 = wA + (set ? 512 : 0) + hq * 64;
            const uint16_t* w1 = wB + (set ? 0 : 512) + hq * 64;
            const uint16_t* w2 = (set ? wA : wB) + 1024 + hq * 64;
#define P1_ISSUE(s)                                                                                            \
    do {                                                                                                       \
        const int kk_ = ((s) + rot) & 7;                                                                       \
        const unsigned b_ = lds0 + ((s) % 3) * P1_STAGE;                                                       \
        asm volatile("s_mov_b32 m0, %7\n\ts_nop 0\n\tglobal_load_lds_dwordx4 %0, %3\n\t"                     \
                     "s_add_u32 m0, m0, 0x400\n\ts_nop 0\n\tglobal_load_lds_dwordx4 %1, %3\n\t"              \
                     "s_mov_b32 m0, %8\n\ts_nop 0\n\tglobal_load_lds_dwordx4 %2, %4\n\t"                     \
                     "s_add_u32 m0, m0, 0x2000\n\ts_nop 0\n\tglobal_load_lds_dwordx4 %2, %5\n\t"             \
                     "s_add_u32 m0, m0, 0x2000\n\ts_nop 0\n\tglobal_load_lds_dwordx4 %2, %6"                  \
                     :: "v"(vx0), "v"(vx1), "v"(vw), "s"(xb + kk_ * 64), "s"(w0 + kk_ * 64 * 1536), "s"(w1 + kk_ * 64 * 1536), \
                        "s"(w2 + kk_ * 64 * 1536), "s"(b_ + wid * 2048), "s"(b_ + 16384 + wid * 1024) : "memory", "scc");   \
    } while (0)
            const int grow = set * 64 + wid * 8 + lr;
            const lds_ptr gxp = lds + grow * 128 + lc * 16, gwp = lds + L_GW + ((lc ^ ((grow >> 1) & 7)) * 16);
            u32x4 gx[8];
#define P1_GATE(s) gx[s] = *(const LAS u32x4*)(gxp + ((s) % 3) * P1_STAGE)
            P1_ISSUE(0); P1_ISSUE(1);
            STAMP(1);
            WAIT_BAR(5); STAMP(2); P1_ISSUE(2); P1_GATE(0);
            WAIT_BAR(5); P1_ISSUE(3); P1_GATE(1);
            WAIT_BAR(5); P1_ISSUE(4); P1_GATE(2);
            WAIT_BAR(5); P1_ISSUE(5); P1_GATE(3);
            WAIT_BAR(5); STAMP(3); P1_ISSUE(6); P1_GATE(4);
            WAIT_BAR(5); P1_ISSUE(7); P1_GATE(5);
            WAIT_BAR(5); P1_GATE(6);
            WAIT_BAR(0); P1_GATE(7);
            STAMP(4);
            LDS_BAR();
            STAMP(5);
            float gs = 0.f;
#pragma unroll
            for (int s = 0; s < 8; ++s) {
                const u32x4 w_ = *(const LAS u32x4*)(gwp + ((s + rot) & 7) * 128);
#pragma unroll
                for (int i = 0; i < 4; ++i) gs = dot2(gx[s][i], w_[i], gs);
            }
            gs += __shfl_xor(gs, 1); gs += __shfl_xor(gs, 2); gs += __shfl_xor(gs, 4);
            if (lc == 0) __builtin_amdgcn_raw_buffer_store_b32(__float_as_uint(1.f / (1.f + __expf(-gs))), wsr, grow * 4, WS_G + (hq * 2048 + rb * 128) * 4, 16);
            token = uniform64(epoch) + 1;
        }
        __builtin_amdgcn_s_setprio(3);
        if (wid == 0) *(LAS u32x4*)(lds + L_V + lane * VS + 1024) = u32x4{0x3f803f80u, 0x3f803f80u, 0x3f803f80u, 0x3f803f80u};
        signal(CNT(C_F1), 12, f1, token, lane);
        if (wid == 0) {
            lds_wait<2>(CNT(C_F1), 12);
            wait32<4>((gu64*)(ws + WS_F1) + (h >> 2) * 32, token, lane);
            lds_arrive(CNT(C_P1), lane);
        } else {
            lds_wait<2>(CNT(C_P1), 1);
        }
        STAMP(6);
        const int qp = wid & 3, kh = wid >> 2, tile = qb * 8 + qp * 2, tb = kh * 32;
        AttRegs R;
        R.qa = __builtin_bit_cast(bf16x8, ld_dev(wsr, lane * 16, WS_QF + (h * 64 + tile) * 1024));
        R.qb = __builtin_bit_cast(bf16x8, ld_dev(wsr, lane * 16 + 1024, WS_QF + (h * 64 + tile) * 1024));
        const bf16x8 kf0 = __builtin_bit_cast(bf16x8, ld_dev(wsr, lane * 16, WS_KF + h * 65536));
        float ga = 0.f, gb = 0.f;
        {
            const int isv = wid & 1;
            const unsigned so = (isv ? WS_VF : WS_KF) + h * 65536;
            if (!kh) {
                const int t0 = ((wid >> 1) & 1) * 4;
                u32x4 v[4];
#pragma unroll
                for (int i = 0; i < 4; ++i) v[i] = ld_dev(wsr, lane * 16 + i * 1024, so + t0 * 1024);
#pragma unroll
                for (int i = 0; i < 4; ++i) *(LAS u32x4*)(kp + (isv * 64 + t0 + i) * VS) = v[i];
                lds_arrive(CNT(C_KV0), lane);
            } else {
                const int t0 = 8 + ((wid >> 1) & 1) * 12, t1 = 32 + ((wid >> 1) & 1) * 16;
                u32x4 v[12], w[16];
#pragma unroll
                for (int i = 0; i < 12; ++i) v[i] = ld_dev(wsr, lane * 16 + (i & 3) * 1024, so + (t0 + (i & 12)) * 1024);
#pragma unroll
                for (int i = 0; i < 8; ++i) w[i] = ld_dev(wsr, lane * 16 + (i & 3) * 1024, so + (t1 + (i & 12)) * 1024);
                SBAR();
#pragma unroll
                for (int i = 0; i < 12; ++i) *(LAS u32x4*)(kp + (isv * 64 + t0 + i) * VS) = v[i];
                lds_arrive(CNT(C_KV1), lane);
#pragma unroll
                for (int i = 8; i < 16; ++i) w[i] = ld_dev(wsr, lane * 16 + (i & 3) * 1024, so + (t1 + (i & 12)) * 1024);
                ga = __uint_as_float(__builtin_amdgcn_raw_buffer_load_b32(wsr, r32 * 4, WS_G + ((h >> 2) * 2048 + tile * 32) * 4, 16));
                gb = __uint_as_float(__builtin_amdgcn_raw_buffer_load_b32(wsr, r32 * 4 + 128, WS_G + ((h >> 2) * 2048 + tile * 32) * 4, 16));
#pragma unroll
                for (int i = 0; i < 16; ++i) *(LAS u32x4*)(kp + (isv * 64 + t1 + i) * VS) = w[i];
                lds_arrive(CNT(C_KV2), lane);
            }
        }
        {
            const float ma = xhalf_max(tile_max(MFMA(kf0, R.qa, z16))), mb = xhalf_max(tile_max(MFMA(kf0, R.qb, z16)));
#pragma unroll
            for (int i = 0; i < 16; ++i) { R.na[i] = -ma; R.nb[i] = -mb; R.oa[i] = 0.f; R.ob[i] = 0.f; }
            asm volatile("" : "+v"(R.na), "+v"(R.nb), "+v"(R.oa), "+v"(R.ob), "+v"(ga), "+v"(gb));
        }
        lds_wait(CNT(kh ? C_KV2 : C_KV0), 4);
        if (kh) __builtin_amdgcn_s_setprio(0); else __builtin_amdgcn_s_setprio(1);
        STAMP(7);
        {
            const bool ones = r32 >= 16 && (r32 < 20 || r32 >= 28);
            const int vo = (hh * 16 + (r32 < 16 ? r32 : 0)) * 16;
            const lds_ptr vp0 = lds + L_V + tb * VS + (ones ? 1024 : vo), vp1 = lds + L_V + tb * VS + (ones ? 1024 : vo + 512);
            R.kp = (unsigned)(uintptr_t)(kp + tb * VS); R.vp0 = (unsigned)(uintptr_t)vp0; R.vp1 = (unsigned)(uintptr_t)vp1;
            R.k0 = ld_b128(kp + tb * VS); R.k1 = ld_b128(kp + tb * VS + VS);
            R.v00 = ld_b128(vp0); R.v01 = ld_b128(vp1);
            asm volatile("" : "=v"(R.sa), "=v"(R.sb), "=v"(R.v10), "=v"(R.v11));
        }
        asm volatile(ATT_HEAD : ATT_OPS(R) : ATT_INS(R) : "memory");
        STAMP(8);
        att_run<0, 6>(R);
        lds_wait(CNT(C_KV1), 4);
        STAMP(9);
        att_run<6, 32>(R);
        STAMP(10);
        RELANE();
        __builtin_amdgcn_s_setprio(3);
        if (kh) {
            lds_wait(CNT(C_EX), 4);
#pragma unroll
            for (int u = 0; u < 2; ++u) {
                f32x16 o = u ? R.ob : R.oa;
                const lds_ptr xp = lds + L_EX + (qp * 2 + u) * 9 * 256;
                const f32x4 e0 = *(const LAS f32x4*)(xp + lane * 16), e1 = *(const LAS f32x4*)(xp + 1024 + lane * 16);
                float l = (hh ? o[12] : o[8]) + *(const LAS float*)(xp + 2048 + lane * 4);
                float chk = 0.f;
#pragma unroll
                for (int i = 0; i < 4; ++i) { o[i] += e0[i]; o[4 + i] += e1[i]; chk += __builtin_fabsf(o[i]) + __builtin_fabsf(o[4 + i]); }
                const bool bad = !(l <= 1.0995116e12f) || !(chk <= 3.0e38f);
                if (__builtin_expect(__any(bad), 0)) {
                    const bf16x8 qf = u ? R.qb : R.qa;
                    float mm = -INFINITY;
#pragma unroll 1
                    for (int t = 0; t < 64; ++t)
                        mm = __builtin_fmaxf(mm, tile_max(MFMA(__builtin_bit_cast(bf16x8, ld_dev(wsr, lane * 16, WS_KF + (h * 64 + t) * 1024)), qf, z16)));
                    mm = xhalf_max(mm);
                    f32x16 negm;
#pragma unroll
                    for (int i = 0; i < 16; ++i) { negm[i] = -mm; o[i] = 0.f; }
#pragma unroll 1
                    for (int t = 0; t < 64; ++t) {
                        f32x16 s = MFMA(__builtin_bit_cast(bf16x8, ld_dev(wsr, lane * 16, WS_KF + (h * 64 + t) * 1024)), qf, negm);
                        u32x4 va = ld_dev(wsr, (hh * 16 + (r32 & 15)) * 16, WS_VF + (h * 64 + t) * 1024), vb = ld_dev(wsr, (hh * 16 + (r32 & 15)) * 16 + 512, WS_VF + (h * 64 + t) * 1024);
#pragma unroll
                        for (int i = 0; i < 4; ++i) { va[i] = r32 < 16 ? va[i] : 0x3f803f80u; vb[i] = r32 < 16 ? vb[i] : 0x3f803f80u; }
#pragma unroll
                        for (int i = 0; i < 16; ++i) s[i] = __builtin_amdgcn_exp2f(s[i]);
                        o = MFMA(__builtin_bit_cast(bf16x8, va), pack8<0>(s), o);
                        o = MFMA(__builtin_bit_cast(bf16x8, vb), pack8<8>(s), o);
                    }
                    l = o[8];
                }
                const float sc = (u ? gb : ga) / l;
#pragma unroll
                for (int i = 0; i < 8; ++i) o[i] *= sc;
                st_wt(wsr, WS_OF + ((tile + u) * 64 + h) * 1024 + lane * 16, pack8u<0>(o));
            }
            STAMP(11);
            __builtin_amdgcn_s_barrier();
            signal(CNT(C_F3), 4, (gu64*)(ws + WS_F3) + qb * 32 + h, token, lane);
            STAMP(12);
            STAMP_OUT(0, 18);
            return;
        }
#pragma unroll
        for (int u = 0; u < 2; ++u) {
            const f32x16& o = u ? R.ob : R.oa;
            const lds_ptr xp = lds + L_EX + (qp * 2 + u) * 9 * 256;
            *(LAS f32x4*)(xp + lane * 16) = f32x4{o[0], o[1], o[2], o[3]};
            *(LAS f32x4*)(xp + 1024 + lane * 16) = f32x4{o[4], o[5], o[6], o[7]};
            *(LAS float*)(xp + 2048 + lane * 4) = hh ? o[12] : o[8];
        }
        lds_arrive(CNT(C_EX), lane);
        STAMP(11);
    } else {
        const int cw = wid - 8, rh = cw & 1, p = cw >> 1;
        if (cw == 3) *(LAS u32x4*)(lds + L_GW + lane * 16) = *(const u32x4*)(gatew + lane * 8);
        const int xsw = (r32 >> 1) & 7;
        lds_ptr xa[2][4], wa[2];
#pragma unroll
        for (int ks = 0; ks < 4; ++ks) {
            xa[0][ks] = lds + (rh * 64 + r32) * 128 + (((2 * ks + hh) ^ xsw) * 16); xa[1][ks] = xa[0][ks] + 2 * P1_STAGE;
            asm volatile("" : "+v"(xa[0][ks]), "+v"(xa[1][ks]));
        }
        const int wch = (p * 4 + g1 * 2 + (pp >> 1)) ^ (((q4 >> 1) & 1) << 2);
        wa[0] = lds + 16384 + (8 * hh + q4) * 128 + wch * 16 + (pp & 1) * 8; wa[1] = wa[0] + 2 * P1_STAGE;
        asm volatile("" : "+v"(wa[0]), "+v"(wa[1]));
        f32x16 a0[2] = {}, a1[2] = {}, a2[2] = {};
        bf16x8 fx[2][2], fw[2][3];
#define P1_READ(n)                                                                               \
    do {                                                                                         \
        const int sl_ = ((n) >> 2) % 3, bo_ = (sl_ & 1) * P1_STAGE, ks_ = (n)&3, z_ = (n)&1, u_ = sl_ >> 1; \
        fx[z_][0] = ld_b128(xa[u_][ks_] + bo_);                                                          \
        fw[z_][0] = ld_tr2(wa[u_] + bo_ + ks_ * 2048, wa[u_] + bo_ + ks_ * 2048 + 512);                  \
        fx[z_][1] = ld_b128(xa[u_][ks_] + bo_ + 4096);                                                   \
        fw[z_][1] = ld_tr2(wa[u_] + bo_ + 8192 + ks_ * 2048, wa[u_] + bo_ + 8192 + ks_ * 2048 + 512);    \
        fw[z_][2] = ld_tr2(wa[u_] + bo_ + 16384 + ks_ * 2048, wa[u_] + bo_ + 16384 + ks_ * 2048 + 512);  \
    } while (0)
        STAMP(1);
        LDS_BAR();
        STAMP(2);
        P1_READ(0);
#pragma unroll
        for (int n = 0; n < 32; ++n) {
            if ((n & 3) == 3) LDS_BAR();
            SBAR();
            if (n < 31) P1_READ(n + 1);
            const int z = n & 1;
            a0[0] = MFMA(fw[z][0], fx[z][0], a0[0]);
            a0[1] = MFMA(fw[z][0], fx[z][1], a0[1]);
            a1[0] = MFMA(fw[z][1], fx[z][0], a1[0]);
            a1[1] = MFMA(fw[z][1], fx[z][1], a1[1]);
            a2[0] = MFMA(fx[z][0], fw[z][2], a2[0]);
            a2[1] = MFMA(fx[z][1], fw[z][2], a2[1]);
            if (n < 31) {
                __builtin_amdgcn_sched_group_barrier(0x8, 1, 0); __builtin_amdgcn_sched_group_barrier(0x100, 2, 0);
                __builtin_amdgcn_sched_group_barrier(0x8, 1, 0); __builtin_amdgcn_sched_group_barrier(0x100, 2, 0);
                __builtin_amdgcn_sched_group_barrier(0x8, 1, 0); __builtin_amdgcn_sched_group_barrier(0x100, 2, 0);
                __builtin_amdgcn_sched_group_barrier(0x8, 1, 0); __builtin_amdgcn_sched_group_barrier(0x100, 2, 0);
                __builtin_amdgcn_sched_group_barrier(0x8, 2, 0);
            }
            SBAR();
        }
        STAMP(4);
        __builtin_amdgcn_s_setprio(3);
        RELANE();
        const u64 epoch = __hip_atomic_load((gu64*)(ws + WS_E), RLX_AGENT);
        const int tile0 = rb * 4 + rh * 2, h0 = hq * 4 + p * 2;
        unsigned wpr[2][8];
#pragma unroll
        for (int e = 0; e < 2; ++e)
#pragma unroll
            for (int j = 0; j < 8; ++j) wpr[e][j] = wphi[(h0 + e) * 256 + (8 * (j >> 2) + 4 * hh + (j & 3)) * 16 + (r32 & 15)];
        unsigned wor[8];
        {
            const int ks = blockIdx.x >> 2;
            const uint16_t* wsrc = (ks < 32 ? woA : woB) + (size_t)((ks & 31) * 16 + 4 * hh) * 512 + ((blockIdx.x & 3) * 4 + cw) * 32 + r32;
#pragma unroll
            for (int j = 0; j < 8; ++j) wor[j] = wsrc[(8 * (j >> 2) + (j & 3)) * 512];
        }
        if (set == 0) {
            const float c = 0.25f * LOG2E;
#pragma unroll
            for (int i = 0; i < 2; ++i) {
#pragma unroll
                for (int j = 0; j < 16; ++j) a0[i][j] *= c;
                const unsigned o = WS_QF + (h0 * 64 + tile0 + i) * 1024 + lane * 16;
                st_wt(wsr, o, pack8u<0>(a0[i])); st_wt(wsr, o + 65536, pack8u<8>(a0[i]));
            }
        } else {
#pragma unroll
            for (int i = 0; i < 2; ++i) {
                const unsigned o = WS_KF + (h0 * 64 + tile0 + i) * 1024 + lane * 16;
                st_wt(wsr, o, pack8u<0>(a0[i])); st_wt(wsr, o + 65536, pack8u<8>(a0[i]));
                const unsigned ov = WS_VF + ((h0 + (r32 >> 4)) * 64 + tile0 + i) * 1024 + (hh * 16 + (r32 & 15)) * 16;
                st_wt(wsr, ov, pack8u<0>(a2[i])); st_wt(wsr, ov + 512, pack8u<8>(a2[i]));
            }
        }
        token = uniform64(epoch) + 1;
        signal(CNT(C_F1), 12, f1, token, lane);
        STAMP(5);
        const unsigned sgn = (r32 & 16) ? 0x80008000u : 0u;
        const lds_ptr tsc = lds + L_TR + cw * KVP;
        u32x4 bo[2][4];
#pragma unroll
        for (int e = 0; e < 2; ++e) {
            u32x4 wu;
#pragma unroll
            for (int i = 0; i < 4; ++i) wu[i] = (wpr[e][2 * i] | (wpr[e][2 * i + 1] << 16)) ^ sgn;
            const bf16x8 wf = __builtin_bit_cast(bf16x8, wu);
            f32x16 t0 = MFMA(wf, e ? pack8<8>(a1[0]) : pack8<0>(a1[0]), z16);
            f32x16 t1 = MFMA(wf, e ? pack8<8>(a1[1]) : pack8<0>(a1[1]), z16);
            dual_softmax2(t0, t1);
            if (set) {
                bo[e][0] = pack8u<0>(t0); bo[e][1] = pack8u<8>(t0); bo[e][2] = pack8u<0>(t1); bo[e][3] = pack8u<8>(t1);
            } else {
                const bool mine = (r32 >> 4) == e;
                f32x16 kv = z16;
#pragma unroll
                for (int i = 0; i < 2; ++i) {
                    const f32x16& tk = i ? t1 : t0;
#pragma unroll
                    for (int g = 0; g < 4; ++g) {
                        u32x2 w2 = {pk2(tk[4 * g], tk[4 * g + 1]), pk2(tk[4 * g + 2], tk[4 * g + 3])};
                        *(LAS u32x2*)(tsc + r32 * 64 + (((2 * g + hh) ^ ((r32 >> 1) & 7)) * 8)) = w2;
                    }
                    bf16x8 af[2];
#pragma unroll
                    for (int ks = 0; ks < 2; ++ks) {
                        const int ra = 16 * ks + 4 * hh + q4, rb_ = ra + 8, sl = g1 * 4 + pp;
                        af[ks] = ld_tr2(tsc + ra * 64 + ((sl ^ ((ra >> 1) & 7)) * 8), tsc + rb_ * 64 + ((sl ^ ((rb_ >> 1) & 7)) * 8));
                    }
                    u32x4 b0 = pack8u<0>(a2[i]), b1 = pack8u<8>(a2[i]);
#pragma unroll
                    for (int j = 0; j < 4; ++j) { b0[j] = mine ? b0[j] : 0x3f803f80u; b1[j] = mine ? b1[j] : 0x3f803f80u; }
                    kv = MFMA(af[0], __builtin_bit_cast(bf16x8, b0), kv);
                    kv = MFMA(af[1], __builtin_bit_cast(bf16x8, b1), kv);
                }
#pragma unroll
                for (int g = 0; g < 4; ++g) bo[e][g] = __builtin_bit_cast(u32x4, f32x4{kv[4 * g], kv[4 * g + 1], kv[4 * g + 2], kv[4 * g + 3]});
            }
        }
        asm volatile("" : "+v"(bo[0][0]), "+v"(bo[0][1]), "+v"(bo[0][2]), "+v"(bo[0][3]), "+v"(bo[1][0]), "+v"(bo[1][1]), "+v"(bo[1][2]), "+v"(bo[1][3]));
        STAMP(9);
        lds_wait<8>(CNT(C_KV2), 4);
        STAMP(10);
#pragma unroll
        for (int e = 0; e < 2; ++e) {
            if (set) {
#pragma unroll
                for (int g = 0; g < 4; ++g) st_wt(wsr, WS_PQ + ((h0 + e) * 64 + tile0) * 2048 + lane * 16 + g * 1024, bo[e][g]);
            } else {
                const bool mine = (r32 >> 4) == e;
                const int col = mine ? (r32 & 15) : 16;
                if (mine || (r32 & 15) == 0) {
#pragma unroll
                    for (int g = 0; g < 4; ++g) st_wt(wsr, WS_KV + ((h0 + e) * 32 + rb * 2 + rh) * KVP + (col * 2 + hh) * 16 + g * 544, bo[e][g]);
                }
            }
        }
        st_wt(wsr, WS_WO + (blockIdx.x * 4 + cw) * 1024 + lane * 16,
              u32x4{wor[0] | (wor[1] << 16), wor[2] | (wor[3] << 16), wor[4] | (wor[5] << 16), wor[6] | (wor[7] << 16)});
        STAMP(6);
        signal(CNT(C_F2), 4, (gu64*)(ws + WS_F2) + hq * 32 + rb * 2 + set, token, lane);
        STAMP(7);
        if (cw == 0) {
            lds_wait<8>(CNT(C_F2), 4);
            wait32<16>((gu64*)(ws + WS_F2) + (h >> 2) * 32, token, lane);
            lds_arrive(CNT(C_P2), lane);
        } else {
            lds_wait<16>(CNT(C_P2), 1);
        }
        STAMP(8);
        {
            const int tile = qb * 8 + cw * 2;
            const unsigned so = WS_KV + (h * 32 + cw * 8) * KVP;
            const unsigned o2 = lane < 8 ? 2048 + lane * 16 : 0;
            f32x4 s0 = {}, s1 = {}, s2 = {};
#pragma unroll
            for (int i = 0; i < 8; ++i) {
                s0 += __builtin_bit_cast(f32x4, ld_dev(wsr, lane * 16, so + i * KVP));
                s1 += __builtin_bit_cast(f32x4, ld_dev(wsr, lane * 16 + 1024, so + i * KVP));
                s2 += __builtin_bit_cast(f32x4, ld_dev(wsr, o2, so + i * KVP));
            }
            u32x4 pq[4];
#pragma unroll
            for (int i = 0; i < 4; ++i) pq[i] = ld_dev(wsr, lane * 16 + i * 1024, WS_PQ + (h * 64 + tile) * 2048);
            const float ga = __uint_as_float(__builtin_amdgcn_raw_buffer_load_b32(wsr, r32 * 4, WS_G + ((h >> 2) * 2048 + tile * 32) * 4, 16));
            const float gb = __uint_as_float(__builtin_amdgcn_raw_buffer_load_b32(wsr, r32 * 4 + 128, WS_G + ((h >> 2) * 2048 + tile * 32) * 4, 16));
            *(LAS f32x4*)(tsc + lane * 16) = s0;
            *(LAS f32x4*)(tsc + 1024 + lane * 16) = s1;
            if (lane < 8) *(LAS f32x4*)(tsc + 2048 + lane * 16) = s2;
            lds_arrive(CNT(C_S0), lane);
            lds_wait<4>(CNT(C_S0), 4);
            f32x16 kv = {};
            const lds_ptr ks = lds + L_TR + ((r32 < 16 ? r32 : 16) * 2 + hh) * 16;
#pragma unroll
            for (int w = 0; w < 4; ++w)
#pragma unroll
                for (int g = 0; g < 4; ++g) {
                    const f32x4 t = *(const LAS f32x4*)(ks + w * KVP + g * 544);
                    kv[4 * g] += t[0]; kv[4 * g + 1] += t[1]; kv[4 * g + 2] += t[2]; kv[4 * g + 3] += t[3];
                }
            const bf16x8 kv0 = pack8<0>(kv), kv1 = pack8<8>(kv);
#pragma unroll
            for (int u = 0; u < 2; ++u) {
                f32x16 nb = MFMA(kv0, __builtin_bit_cast(bf16x8, pq[2 * u]), z16);
                nb = MFMA(kv1, __builtin_bit_cast(bf16x8, pq[2 * u + 1]), nb);
                const float sc = (1.f - (u ? gb : ga)) / (nb[8] + 1e-6f);
#pragma unroll
                for (int i = 0; i < 8; ++i) nb[i] *= sc;
                st_wt(wsr, WS_OF + ((tile + u) * 64 + 32 + h) * 1024 + lane * 16, pack8u<0>(nb));
            }
            signal(CNT(C_F4), 4, (gu64*)(ws + WS_F4) + qb * 32 + h, token, lane);
        }
        STAMP(11);
    }

    RELANE();
    const int j8 = wid < 4 ? wid : wid - 4, kq = j8 >> 1, rt = j8 & 1;
    f32x16 acc[2] = {};
    if (wid == 8) { wait32<32>((gu64*)(ws + WS_F4) + (rb3 >> 2) * 32, token, lane); lds_arrive(CNT(C_P4), lane); }
    else lds_wait<32>(CNT(C_P4), 1);
#pragma unroll 1
    for (int br = 1; br >= 0; --br) {
        u32x4 wf[8][2];
#pragma unroll
        for (int i = 0; i < 8; ++i) {
            wf[i][0] = ld_dev(wsr, lane * 16, WS_WO + ((br * 32 + kq * 8 + i) * 16 + cb * 2) * 1024);
            wf[i][1] = ld_dev(wsr, lane * 16 + 1024, WS_WO + ((br * 32 + kq * 8 + i) * 16 + cb * 2) * 1024);
        }
        if (br == 0) {
            STAMP(13);
            LDS_BAR();
            if (wid == 8) wait32<2>((gu64*)(ws + WS_F3) + (rb3 >> 2) * 32, token, lane);
            LDS_BAR();
            STAMP(14);
        }
        u32x4 of[8];
#pragma unroll
        for (int i = 0; i < 8; ++i) of[i] = ld_dev(wsr, lane * 16 + (i & 3) * 1024, WS_OF + ((rb3 * 2 + rt) * 64 + br * 32 + kq * 8 + (i & 4)) * 1024);
#pragma unroll
        for (int i = 0; i < 8; ++i) {
            acc[0] = MFMA(__builtin_bit_cast(bf16x8, wf[i][0]), __builtin_bit_cast(bf16x8, of[i]), acc[0]);
            acc[1] = MFMA(__builtin_bit_cast(bf16x8, wf[i][1]), __builtin_bit_cast(bf16x8, of[i]), acc[1]);
        }
        if (br == 1) STAMP(12);
    }
    STAMP(15);
    if (blockIdx.x == 0 && tid == 0) __hip_atomic_store((gu64*)(ws + WS_E), token, RLX_AGENT);
    {
        const lds_ptr d = lds + (kq >> 1) * L_V + (kq & 1) * 16384 + (rt * 32 + r32) * 256;
#pragma unroll
        for (int ct = 0; ct < 2; ++ct)
#pragma unroll
            for (int q = 0; q < 4; ++q)
                *(LAS f32x4*)(d + (((ct * 8 + q * 2 + hh) ^ (r32 & 15)) * 16)) = f32x4{acc[ct][4 * q], acc[ct][4 * q + 1], acc[ct][4 * q + 2], acc[ct][4 * q + 3]};
    }
    LDS_BAR();
    STAMP(16);
    {
        const int row = j8 * 8 + lr;
        const lds_ptr s0 = lds + row * 256 + (((2 * lc) ^ (row & 15)) * 16), s1 = lds + row * 256 + (((2 * lc + 1) ^ (row & 15)) * 16);
        f32x4 a0 = {}, a1 = {};
#pragma unroll
        for (int k = 0; k < 4; ++k) { const int o = (k >> 1) * L_V + (k & 1) * 16384; a0 += *(const LAS f32x4*)(s0 + o); a1 += *(const LAS f32x4*)(s1 + o); }
        u32x4 r = {pk2(a0[0], a0[1]), pk2(a0[2], a0[3]), pk2(a1[0], a1[1]), pk2(a1[2], a1[3])};
        *(u32x4*)(out + (size_t)(rb3 * 64 + row) * 512 + cb * 64 + lc * 8) = r;
    }
    STAMP(17);
    STAMP_OUT(0, 18);
}

extern "C" void kernel_launch(void* const* d_in, const int* in_sizes, int n_in, void* d_out, int out_size, void* d_ws,
                              size_t ws_size, hipStream_t stream) {
    (void)hipFuncSetAttribute((const void*)fused, hipFuncAttributeMaxDynamicSharedMemorySize, LDS_BYTES);
    fused<<<256, 768, LDS_BYTES, stream>>>((const uint16_t*)d_in[0], (const uint16_t*)d_in[1], (const uint16_t*)d_in[2],
                                           (const uint16_t*)d_in[4], (const uint16_t*)d_in[5], (const uint16_t*)d_in[6],
                                           (const uint16_t*)d_in[7], (uint16_t*)d_out, (char*)d_ws);
}
```

```cpp
#include <hip/hip_runtime.h>
#include <hip/hip_bf16.h>
#include <stdint.h>

typedef __bf16 bf16x8 __attribute__((ext_vector_type(8)));
typedef __bf16 bf16x2 __attribute__((ext_vector_type(2)));
typedef short s16x4 __attribute__((ext_vector_type(4)));
typedef float f32x16 __attribute__((ext_vector_type(16)));
typedef float f32x4 __attribute__((ext_vector_type(4)));
typedef float f32x2 __attribute__((ext_vector_type(2)));
typedef unsigned u32x4 __attribute__((ext_vector_type(4)));
typedef unsigned u32x2 __attribute__((ext_vector_type(2)));
typedef unsigned long long u64;
#define LAS __attribute__((address_space(3)))
#define GAS __attribute__((address_space(1)))
typedef LAS char* lds_ptr;
typedef GAS u64 gu64;
#define RLX_AGENT __ATOMIC_RELAXED, __HIP_MEMORY_SCOPE_AGENT

#define MFMA(a, b, c) __builtin_amdgcn_mfma_f32_32x32x16_bf16((a), (b), (c), 0, 0, 0)
#define WAIT_BAR(N) asm volatile("s_waitcnt vmcnt(" #N ") lgkmcnt(0)\n\ts_barrier" ::: "memory")
#define LDS_BAR() asm volatile("s_waitcnt lgkmcnt(0)\n\ts_barrier" ::: "memory")
#define SBAR() __builtin_amdgcn_sched_barrier(0)

constexpr float LOG2E = 1.4426950408889634f;

constexpr unsigned WS_E = 0;
constexpr unsigned FS = 16;
constexpr unsigned WS_F1 = (20u << 20);
constexpr unsigned WS_F2 = WS_F1 + 256 * FS * 8;
constexpr unsigned WS_F4 = WS_F2 + 256 * FS * 8;
constexpr unsigned WS_F3 = WS_F4 + 256 * FS * 8;
constexpr unsigned WS_G = 65536;
constexpr unsigned WS_QF = 131072;
constexpr unsigned WS_KF = WS_QF + (2u << 20);
constexpr unsigned WS_VF = WS_KF + (2u << 20);
constexpr unsigned WS_PQ = WS_VF + (2u << 20);
constexpr unsigned WS_OF = WS_PQ + (4u << 20);
constexpr unsigned KVP = 2176;
constexpr unsigned WS_KV = WS_OF + (4u << 20);
constexpr unsigned WS_WO = WS_KV + 32 * 32 * KVP;
constexpr unsigned WS_END = WS_WO + (1u << 20);

constexpr int VS = 1040, L_V = 64 * VS;
constexpr int SLOT = 32 * VS;
constexpr int L_X = L_V + 64 * VS;
constexpr int L_CNT = L_X + 2048, L_GW = L_CNT + 256;
constexpr int L_G = L_GW + 1024;
constexpr int L_WP = L_G + 1024;
constexpr int L_TR = L_WP + 2048;
constexpr int L_PK = 18 * VS + 16, L_PK7 = L_TR + 16384;
constexpr int L_TELL = L_PK7 + 4096;
constexpr int LDS_BYTES = 163840;
static_assert(L_TELL + 1024 <= LDS_BYTES && L_PK + 7 * 2048 <= SLOT && WS_END <= WS_F1, "maps");
enum { C_F1 = 0, C_F2, C_F4, C_S0, C_P1, C_P2, C_P4, C_W1, C_PK, C_PD, C_PW, C_PG = 16, C_KV = C_PG + 8 };
__host__ __device__ constexpr int slot_of(int s) { return ((s & 1) * 2 + 1 - ((s >> 1) & 1)) * SLOT; }

__device__ __forceinline__ void glds16(const void* g, unsigned lds_base) {
    unsigned sv;
    asm volatile("s_mov_b32 %0, m0\n\ts_mov_b32 m0, %2\n\ts_nop 0\n\tglobal_load_lds_dwordx4 %1, off\n\ts_mov_b32 m0, %0"
                 : "=&s"(sv) : "v"(g), "s"(lds_base) : "memory");
}
__device__ __forceinline__ unsigned pk2(float lo, float hi) {
    f32x2 v = {lo, hi};
    return __builtin_bit_cast(unsigned, __builtin_convertvector(v, bf16x2));
}
template <int O>
__device__ __forceinline__ u32x4 pack8u(const f32x16& a) {
    return u32x4{pk2(a[O], a[O + 1]), pk2(a[O + 2], a[O + 3]), pk2(a[O + 4], a[O + 5]), pk2(a[O + 6], a[O + 7])};
}
template <int O>
__device__ __forceinline__ bf16x8 pack8(const f32x16& a) { return __builtin_bit_cast(bf16x8, pack8u<O>(a)); }
__device__ __forceinline__ bf16x8 ld_b128(lds_ptr p) { return *(const LAS bf16x8*)p; }
__device__ __forceinline__ u32x2 ld_tr(lds_ptr p) {
    return __builtin_bit_cast(u32x2, __builtin_amdgcn_ds_read_tr16_b64_v4i16((LAS s16x4*)p));
}
__device__ __forceinline__ bf16x8 ld_tr2(lds_ptr p0, lds_ptr p1) {
    u32x2 a = ld_tr(p0), b = ld_tr(p1);
    u32x4 r = {a[0], a[1], b[0], b[1]};
    return __builtin_bit_cast(bf16x8, r);
}
__device__ __forceinline__ float xhalf_max(float m) {
    auto r = __builtin_amdgcn_permlane32_swap(__float_as_uint(m), __float_as_uint(m), false, false);
    return __builtin_fmaxf(__uint_as_float(r[0]), __uint_as_float(r[1]));
}
#define MX3(a, b, c) __builtin_fmaxf(__builtin_fmaxf((a), (b)), (c))
typedef __amdgpu_buffer_rsrc_t rsrc_t;
__device__ __forceinline__ void st_wt(rsrc_t r, unsigned off, u32x4 v) { __builtin_amdgcn_raw_buffer_store_b128(v, r, off, 0, 16); }
__device__ __forceinline__ u32x4 ld_dev(rsrc_t r, unsigned off, unsigned soff) { return __builtin_amdgcn_raw_buffer_load_b128(r, off, soff, 16); }

__device__ __forceinline__ u64 uniform64(u64 v) {
    return ((u64)(unsigned)__builtin_amdgcn_readfirstlane((unsigned)(v >> 32)) << 32) | (unsigned)__builtin_amdgcn_readfirstlane((unsigned)v);
}
__device__ __forceinline__ unsigned lds_add1(lds_ptr cnt) {
    unsigned r;
    asm volatile("s_waitcnt lgkmcnt(0)\n\ts_mov_b64 exec, 1\n\tds_add_rtn_u32 %0, %1, %2\n\ts_mov_b64 exec, -1\n\ts_waitcnt lgkmcnt(0)"
                 : "=&v"(r) : "v"((unsigned)(uintptr_t)cnt), "v"(1u) : "memory");
    return __builtin_amdgcn_readfirstlane(r);
}
__device__ __forceinline__ void signal(lds_ptr cnt, unsigned n, gu64* flag, u64 token, int lane) {
    asm volatile("s_waitcnt vmcnt(0)" ::: "memory");
    if (lds_add1(cnt) == n - 1 && lane == 0) __hip_atomic_store(flag, token, RLX_AGENT);
}
__device__ __forceinline__ void lds_arrive(lds_ptr cnt, int lane) {
    asm volatile("s_waitcnt lgkmcnt(0)\n\ts_mov_b64 exec, 1\n\tds_add_u32 %0, %1\n\ts_mov_b64 exec, -1"
                 :: "v"((unsigned)(uintptr_t)cnt), "v"(1u) : "memory");
}
template <int SLEEP = 1>
__device__ __forceinline__ void lds_wait(lds_ptr cnt, unsigned n) {
#pragma unroll 1
    for (unsigned spins = 0; spins < (1u << 24); ++spins) {
        if ((unsigned)__builtin_amdgcn_readfirstlane(__hip_atomic_load((LAS unsigned*)cnt, __ATOMIC_RELAXED, __HIP_MEMORY_SCOPE_WORKGROUP)) >= n) break;
        __builtin_amdgcn_s_sleep(SLEEP);
    }
    asm volatile("" ::: "memory");
}
template <int SLEEP>
__device__ __forceinline__ void wait32(gu64* flags, u64 token, int lane) {
#pragma unroll 1
    for (unsigned spins = 0; spins < (1u << 20); ++spins) {
        const u64 v = __hip_atomic_load(flags + (lane & 31) * FS, RLX_AGENT);
        if (__all(v == token)) break;
        __builtin_amdgcn_s_sleep(SLEEP);
    }
    __builtin_amdgcn_fence(__ATOMIC_ACQUIRE, "wavefront");
    asm volatile("" ::: "memory");
}

__device__ __forceinline__ float max3(float a, float b, float c) { float r; asm("v_max3_f32 %0, %1, %2, %3" : "=v"(r) : "v"(a), "v"(b), "v"(c)); return r; }
__device__ __forceinline__ float max3p(float a, float b, float c) {
    float r; asm("v_max3_f32 %0, %1, %2, %3\n\ts_nop 1" : "=v"(r) : "v"(a), "v"(b), "v"(c)); return r;
}
__device__ __forceinline__ void dual_softmax2(f32x16& t, f32x16& u) {
    asm volatile("s_nop 7\n\ts_nop 3" : "+v"(t), "+v"(u));
    float m[4];
#pragma unroll
    for (int g = 0; g < 4; ++g) {
        const f32x16& a = g & 2 ? u : t; const int o = 8 * (g & 1);
        m[g] = max3p(max3(a[o], a[o + 1], a[o + 2]), max3(a[o + 3], a[o + 4], a[o + 5]), max3(a[o + 6], a[o + 7], a[o + 7]));
    }
#pragma unroll
    for (int g = 0; g < 4; g += 2) {
        auto r = __builtin_amdgcn_permlane32_swap(__float_as_uint(m[g]), __float_as_uint(m[g + 1]), false, false);
        const float x = -LOG2E * max3(__uint_as_float(r[0]), __uint_as_float(r[1]), __uint_as_float(r[1]));
        auto q = __builtin_amdgcn_permlane32_swap(__float_as_uint(x), __float_as_uint(x), false, false);
        m[g] = __uint_as_float(q[0]); m[g + 1] = __uint_as_float(q[1]);
    }
    const f32x2 c2 = {LOG2E, LOG2E};
#pragma unroll
    for (int i = 0; i < 16; i += 2) {
        const f32x2 a = __builtin_elementwise_fma(f32x2{t[i], t[i + 1]}, c2, f32x2{m[i >> 3], m[i >> 3]});
        const f32x2 b = __builtin_elementwise_fma(f32x2{u[i], u[i + 1]}, c2, f32x2{m[2 + (i >> 3)], m[2 + (i >> 3)]});
        t[i] = __builtin_amdgcn_exp2f(a[0]); t[i + 1] = __builtin_amdgcn_exp2f(a[1]);
        u[i] = __builtin_amdgcn_exp2f(b[0]); u[i + 1] = __builtin_amdgcn_exp2f(b[1]);
    }
#pragma unroll
    for (int g = 0; g < 4; ++g) {
        const f32x16& a = g & 2 ? u : t; const int o = 8 * (g & 1);
        const f32x2 s2 = (f32x2{a[o], a[o + 1]} + f32x2{a[o + 2], a[o + 3]}) + (f32x2{a[o + 4], a[o + 5]} + f32x2{a[o + 6], a[o + 7]});
        m[g] = s2[0] + s2[1];
    }
#pragma unroll
    for (int g = 0; g < 4; g += 2) {
        auto r = __builtin_amdgcn_permlane32_swap(__float_as_uint(m[g]), __float_as_uint(m[g + 1]), false, false);
        const float x = __builtin_amdgcn_rcpf(__uint_as_float(r[0]) + __uint_as_float(r[1]));
        auto q = __builtin_amdgcn_permlane32_swap(__float_as_uint(x), __float_as_uint(x), false, false);
        m[g] = __uint_as_float(q[0]); m[g + 1] = __uint_as_float(q[1]);
    }
#pragma unroll
    for (int i = 0; i < 8; ++i) { t[i] *= m[0]; t[8 + i] *= m[1]; u[i] *= m[2]; u[8 + i] *= m[3]; }
}
__device__ __forceinline__ float tile_max(const f32x16& s) {
    float a = MX3(s[0], s[1], s[2]), b = MX3(s[3], s[4], s[5]);
    a = MX3(a, s[6], s[7]); b = MX3(b, s[8], s[9]);
    a = MX3(a, s[10], s[11]); b = MX3(b, s[12], s[13]);
    a = MX3(a, s[14], s[15]);
    return __builtin_fmaxf(a, b);
}

#define ATT_HEAD \
    "v_mov_b32 v45, v44\n\t" \
    "v_mov_b64 v[46:47], v[44:45]\n\t" \
    "v_mov_b64 v[48:49], v[44:45]\n\t" \
    "v_mov_b64 v[50:51], v[44:45]\n\t" \
    "v_mov_b64 v[52:53], v[44:45]\n\t" \
    "v_mov_b64 v[54:55], v[44:45]\n\t" \
    "v_mov_b64 v[56:57], v[44:45]\n\t" \
    "v_mov_b64 v[58:59], v[44:45]\n\t" \
    "v_mov_b32 v61, v60\n\t" \
    "v_mov_b64 v[62:63], v[60:61]\n\t" \
    "v_mov_b64 v[64:65], v[60:61]\n\t" \
    "v_mov_b64 v[66:67], v[60:61]\n\t" \
    "v_mov_b64 v[68:69], v[60:61]\n\t" \
    "v_mov_b64 v[70:71], v[60:61]\n\t" \
    "v_mov_b64 v[72:73], v[60:61]\n\t" \
    "v_mov_b64 v[74:75], v[60:61]\n\t" \
    "s_nop 1\n\t" \
    "v_mfma_f32_32x32x16_bf16 v[76:91], v[140:143], v[36:39], v[44:59]\n\t" \
    "v_mfma_f32_32x32x16_bf16 v[92:107], v[140:143], v[40:43], v[60:75]\n\t"
#define ATT_LOOP \
    "s_mov_b32 %[n], 0\n\t" \
    "s_mov_b32 %[sp], 0\n\t" \
    "ds_read_b32 %[tv], %[pa] offset:32\n\t" \
    "s_nop 7\n\t" \
    "v_exp_f32 v76, v76\n\t" \
    "v_exp_f32 v77, v77\n\t" \
    "v_exp_f32 v78, v78\n\t" \
    "v_exp_f32 v79, v79\n\t" \
    "v_exp_f32 v80, v80\n\t" \
    "v_exp_f32 v81, v81\n\t" \
    "v_exp_f32 v82, v82\n\t" \
    "v_exp_f32 v83, v83\n\t" \
    "s_branch LW1_%=\n\t" \
    "LW0_%=:\n\t" \
    "ds_read_b32 %[tv], %[pa] offset:32\n\t" \
    "LW1_%=:\n\t" \
    "s_waitcnt lgkmcnt(0)\n\t" \
    "v_readfirstlane_b32 %[p], %[tv]\n\t" \
    "s_add_u32 %[sp], %[sp], 1\n\t" \
    "s_lshr_b32 %[q], %[sp], 22\n\t" \
    "s_add_u32 %[p], %[p], %[q]\n\t" \
    "s_cmp_lt_u32 %[p], 4\n\t" \
    "s_cbranch_scc1 LW0_%=\n\t" \
    "ds_read_b128 v[148:151], v165\n\t" \
    "ds_read_b128 v[152:155], v166\n\t" \
    "ds_read_b128 v[144:147], v164 offset:1040\n\t" \
    "s_waitcnt lgkmcnt(0)\n\t" \
    "LT_%=:\n\t" \
    "v_exp_f32 v84, v84\n\t" \
    "v_exp_f32 v85, v85\n\t" \
    "v_smfmac_f32_32x32x32_bf16 v[108:123], v[148:151], v[76:83], %[ix]\n\t" \
    "ds_read_b128 v[156:159], v165 offset:1040\n\t" \
    "v_exp_f32 v86, v86\n\t" \
    "v_exp_f32 v87, v87\n\t" \
    "v_exp_f32 v88, v88\n\t" \
    "v_exp_f32 v89, v89\n\t" \
    "v_exp_f32 v90, v90\n\t" \
    "v_exp_f32 v91, v91\n\t" \
    "v_exp_f32 v92, v92\n\t" \
    "v_exp_f32 v93, v93\n\t" \
    "v_smfmac_f32_32x32x32_bf16 v[108:123], v[152:155], v[84:91], %[ix]\n\t" \
    "ds_read_b128 v[160:163], v166 offset:1040\n\t" \
    "v_exp_f32 v94, v94\n\t" \
    "v_exp_f32 v95, v95\n\t" \
    "v_exp_f32 v96, v96\n\t" \
    "v_mfma_f32_32x32x16_bf16 v[76:91], v[144:147], v[36:39], v[44:59]\n\t" \
    "ds_read_b128 v[140:143], v164 offset:2080\n\t" \
    "v_exp_f32 v97, v97\n\t" \
    "v_exp_f32 v98, v98\n\t" \
    "v_exp_f32 v99, v99\n\t" \
    "v_exp_f32 v100, v100\n\t" \
    "v_exp_f32 v101, v101\n\t" \
    "v_smfmac_f32_32x32x32_bf16 v[124:139], v[148:151], v[92:99], %[ix]\n\t" \
    "v_exp_f32 v102, v102\n\t" \
    "v_exp_f32 v103, v103\n\t" \
    "v_exp_f32 v104, v104\n\t" \
    "v_exp_f32 v105, v105\n\t" \
    "v_exp_f32 v106, v106\n\t" \
    "v_exp_f32 v107, v107\n\t" \
    "v_exp_f32 v76, v76\n\t" \
    "v_exp_f32 v77, v77\n\t" \
    "v_smfmac_f32_32x32x32_bf16 v[124:139], v[152:155], v[100:107], %[ix]\n\t" \
    "v_exp_f32 v78, v78\n\t" \
    "v_exp_f32 v79, v79\n\t" \
    "v_exp_f32 v80, v80\n\t" \
    "v_mfma_f32_32x32x16_bf16 v[92:107], v[144:147], v[40:43], v[60:75]\n\t" \
    "s_waitcnt lgkmcnt(0)\n\t" \
    "v_exp_f32 v81, v81\n\t" \
    "v_exp_f32 v82, v82\n\t" \
    "v_exp_f32 v83, v83\n\t" \
    "v_exp_f32 v84, v84\n\t" \
    "v_exp_f32 v85, v85\n\t" \
    "v_smfmac_f32_32x32x32_bf16 v[108:123], v[156:159], v[76:83], %[ix]\n\t" \
    "ds_read_b128 v[148:151], v165 offset:2080\n\t" \
    "v_exp_f32 v86, v86\n\t" \
    "v_exp_f32 v87, v87\n\t" \
    "v_exp_f32 v88, v88\n\t" \
    "v_exp_f32 v89, v89\n\t" \
    "v_exp_f32 v90, v90\n\t" \
    "v_exp_f32 v91, v91\n\t" \
    "v_exp_f32 v92, v92\n\t" \
    "v_exp_f32 v93, v93\n\t" \
    "v_smfmac_f32_32x32x32_bf16 v[108:123], v[160:163], v[84:91], %[ix]\n\t" \
    "ds_read_b128 v[152:155], v166 offset:2080\n\t" \
    "v_exp_f32 v94, v94\n\t" \
    "v_exp_f32 v95, v95\n\t" \
    "v_exp_f32 v96, v96\n\t" \
    "v_mfma_f32_32x32x16_bf16 v[76:91], v[140:143], v[36:39], v[44:59]\n\t" \
    "ds_read_b128 v[144:147], v164 offset:3120\n\t" \
    "v_exp_f32 v97, v97\n\t" \
    "v_exp_f32 v98, v98\n\t" \
    "v_exp_f32 v99, v99\n\t" \
    "v_exp_f32 v100, v100\n\t" \
    "v_exp_f32 v101, v101\n\t" \
    "v_smfmac_f32_32x32x32_bf16 v[124:139], v[156:159], v[92:99], %[ix]\n\t" \
    "v_exp_f32 v102, v102\n\t" \
    "v_exp_f32 v103, v103\n\t" \
    "v_exp_f32 v104, v104\n\t" \
    "v_exp_f32 v105, v105\n\t" \
    "v_exp_f32 v106, v106\n\t" \
    "v_exp_f32 v107, v107\n\t" \
    "v_exp_f32 v76, v76\n\t" \
    "v_exp_f32 v77, v77\n\t" \
    "v_smfmac_f32_32x32x32_bf16 v[124:139], v[160:163], v[100:107], %[ix]\n\t" \
    "v_exp_f32 v78, v78\n\t" \
    "v_exp_f32 v79, v79\n\t" \
    "v_exp_f32 v80, v80\n\t" \
    "v_mfma_f32_32x32x16_bf16 v[92:107], v[140:143], v[40:43], v[60:75]\n\t" \
    "s_waitcnt lgkmcnt(0)\n\t" \
    "v_exp_f32 v81, v81\n\t" \
    "v_exp_f32 v82, v82\n\t" \
    "v_exp_f32 v83, v83\n\t" \
    "v_exp_f32 v84, v84\n\t" \
    "v_exp_f32 v85, v85\n\t" \
    "v_smfmac_f32_32x32x32_bf16 v[108:123], v[148:151], v[76:83], %[ix]\n\t" \
    "ds_read_b128 v[156:159], v165 offset:3120\n\t" \
    "v_exp_f32 v86, v86\n\t" \
    "v_exp_f32 v87, v87\n\t" \
    "v_exp_f32 v88, v88\n\t" \
    "v_exp_f32 v89, v89\n\t" \
    "v_exp_f32 v90, v90\n\t" \
    "v_exp_f32 v91, v91\n\t" \
    "v_exp_f32 v92, v92\n\t" \
    "v_exp_f32 v93, v93\n\t" \
    "v_smfmac_f32_32x32x32_bf16 v[108:123], v[152:155], v[84:91], %[ix]\n\t" \
    "ds_read_b128 v[160:163], v166 offset:3120\n\t" \
    "v_exp_f32 v94, v94\n\t" \
    "v_exp_f32 v95, v95\n\t" \
    "v_exp_f32 v96, v96\n\t" \
    "v_mfma_f32_32x32x16_bf16 v[76:91], v[144:147], v[36:39], v[44:59]\n\t" \
    "ds_read_b128 v[140:143], v164 offset:4160\n\t" \
    "v_exp_f32 v97, v97\n\t" \
    "v_exp_f32 v98, v98\n\t" \
    "v_exp_f32 v99, v99\n\t" \
    "v_exp_f32 v100, v100\n\t" \
    "v_exp_f32 v101, v101\n\t" \
    "v_smfmac_f32_32x32x32_bf16 v[124:139], v[148:151], v[92:99], %[ix]\n\t" \
    "ds_write_b32 %[pb], v164\n\t" \
    "v_exp_f32 v102, v102\n\t" \
    "v_exp_f32 v103, v103\n\t" \
    "v_exp_f32 v104, v104\n\t" \
    "v_exp_f32 v105, v105\n\t" \
    "v_exp_f32 v106, v106\n\t" \
    "v_exp_f32 v107, v107\n\t" \
    "v_exp_f32 v76, v76\n\t" \
    "v_exp_f32 v77, v77\n\t" \
    "v_smfmac_f32_32x32x32_bf16 v[124:139], v[152:155], v[100:107], %[ix]\n\t" \
    "v_exp_f32 v78, v78\n\t" \
    "v_exp_f32 v79, v79\n\t" \
    "v_exp_f32 v80, v80\n\t" \
    "v_mfma_f32_32x32x16_bf16 v[92:107], v[144:147], v[40:43], v[60:75]\n\t" \
    "s_waitcnt lgkmcnt(0)\n\t" \
    "v_exp_f32 v81, v81\n\t" \
    "v_exp_f32 v82, v82\n\t" \
    "v_exp_f32 v83, v83\n\t" \
    "v_exp_f32 v84, v84\n\t" \
    "v_exp_f32 v85, v85\n\t" \
    "v_smfmac_f32_32x32x32_bf16 v[108:123], v[156:159], v[76:83], %[ix]\n\t" \
    "ds_read_b128 v[148:151], v165 offset:4160\n\t" \
    "v_exp_f32 v86, v86\n\t" \
    "v_exp_f32 v87, v87\n\t" \
    "v_exp_f32 v88, v88\n\t" \
    "v_exp_f32 v89, v89\n\t" \
    "v_exp_f32 v90, v90\n\t" \
    "v_exp_f32 v91, v91\n\t" \
    "v_exp_f32 v92, v92\n\t" \
    "v_exp_f32 v93, v93\n\t" \
    "v_smfmac_f32_32x32x32_bf16 v[108:123], v[160:163], v[84:91], %[ix]\n\t" \
    "ds_read_b128 v[152:155], v166 offset:4160\n\t" \
    "v_exp_f32 v94, v94\n\t" \
    "v_exp_f32 v95, v95\n\t" \
    "v_exp_f32 v96, v96\n\t" \
    "v_mfma_f32_32x32x16_bf16 v[76:91], v[140:143], v[36:39], v[44:59]\n\t" \
    "ds_read_b128 v[144:147], v164 offset:5200\n\t" \
    "v_exp_f32 v97, v97\n\t" \
    "v_exp_f32 v98, v98\n\t" \
    "v_exp_f32 v99, v99\n\t" \
    "v_exp_f32 v100, v100\n\t" \
    "v_exp_f32 v101, v101\n\t" \
    "v_smfmac_f32_32x32x32_bf16 v[124:139], v[156:159], v[92:99], %[ix]\n\t" \
    "v_exp_f32 v102, v102\n\t" \
    "v_exp_f32 v103, v103\n\t" \
    "v_exp_f32 v104, v104\n\t" \
    "v_exp_f32 v105, v105\n\t" \
    "v_exp_f32 v106, v106\n\t" \
    "v_exp_f32 v107, v107\n\t" \
    "v_exp_f32 v76, v76\n\t" \
    "v_exp_f32 v77, v77\n\t" \
    "v_smfmac_f32_32x32x32_bf16 v[124:139], v[160:163], v[100:107], %[ix]\n\t" \
    "v_exp_f32 v78, v78\n\t" \
    "v_exp_f32 v79, v79\n\t" \
    "v_exp_f32 v80, v80\n\t" \
    "v_mfma_f32_32x32x16_bf16 v[92:107], v[140:143], v[40:43], v[60:75]\n\t" \
    "s_waitcnt lgkmcnt(0)\n\t" \
    "v_exp_f32 v81, v81\n\t" \
    "v_exp_f32 v82, v82\n\t" \
    "v_exp_f32 v83, v83\n\t" \
    "v_exp_f32 v84, v84\n\t" \
    "v_exp_f32 v85, v85\n\t" \
    "v_smfmac_f32_32x32x32_bf16 v[108:123], v[148:151], v[76:83], %[ix]\n\t" \
    "ds_read_b128 v[156:159], v165 offset:5200\n\t" \
    "v_exp_f32 v86, v86\n\t" \
    "v_exp_f32 v87, v87\n\t" \
    "v_exp_f32 v88, v88\n\t" \
    "v_exp_f32 v89, v89\n\t" \
    "v_exp_f32 v90, v90\n\t" \
    "v_exp_f32 v91, v91\n\t" \
    "v_exp_f32 v92, v92\n\t" \
    "v_exp_f32 v93, v93\n\t" \
    "v_smfmac_f32_32x32x32_bf16 v[108:123], v[152:155], v[84:91], %[ix]\n\t" \
    "ds_read_b128 v[160:163], v166 offset:5200\n\t" \
    "v_exp_f32 v94, v94\n\t" \
    "v_exp_f32 v95, v95\n\t" \
    "v_exp_f32 v96, v96\n\t" \
    "v_mfma_f32_32x32x16_bf16 v[76:91], v[144:147], v[36:39], v[44:59]\n\t" \
    "ds_read_b128 v[140:143], v164 offset:6240\n\t" \
    "v_exp_f32 v97, v97\n\t" \
    "v_exp_f32 v98, v98\n\t" \
    "v_exp_f32 v99, v99\n\t" \
    "v_exp_f32 v100, v100\n\t" \
    "v_exp_f32 v101, v101\n\t" \
    "v_smfmac_f32_32x32x32_bf16 v[124:139], v[148:151], v[92:99], %[ix]\n\t" \
    "v_exp_f32 v102, v102\n\t" \
    "v_exp_f32 v103, v103\n\t" \
    "v_exp_f32 v104, v104\n\t" \
    "v_exp_f32 v105, v105\n\t" \
    "v_exp_f32 v106, v106\n\t" \
    "v_exp_f32 v107, v107\n\t" \
    "v_exp_f32 v76, v76\n\t" \
    "v_exp_f32 v77, v77\n\t" \
    "v_smfmac_f32_32x32x32_bf16 v[124:139], v[152:155], v[100:107], %[ix]\n\t" \
    "v_exp_f32 v78, v78\n\t" \
    "v_exp_f32 v79, v79\n\t" \
    "v_exp_f32 v80, v80\n\t" \
    "v_mfma_f32_32x32x16_bf16 v[92:107], v[144:147], v[40:43], v[60:75]\n\t" \
    "s_waitcnt lgkmcnt(0)\n\t" \
    "v_exp_f32 v81, v81\n\t" \
    "v_exp_f32 v82, v82\n\t" \
    "v_exp_f32 v83, v83\n\t" \
    "v_exp_f32 v84, v84\n\t" \
    "v_exp_f32 v85, v85\n\t" \
    "v_smfmac_f32_32x32x32_bf16 v[108:123], v[156:159], v[76:83], %[ix]\n\t" \
    "ds_read_b128 v[148:151], v165 offset:6240\n\t" \
    "v_exp_f32 v86, v86\n\t" \
    "v_exp_f32 v87, v87\n\t" \
    "v_exp_f32 v88, v88\n\t" \
    "v_exp_f32 v89, v89\n\t" \
    "v_exp_f32 v90, v90\n\t" \
    "v_exp_f32 v91, v91\n\t" \
    "v_exp_f32 v92, v92\n\t" \
    "v_exp_f32 v93, v93\n\t" \
    "v_smfmac_f32_32x32x32_bf16 v[108:123], v[160:163], v[84:91], %[ix]\n\t" \
    "ds_read_b128 v[152:155], v166 offset:6240\n\t" \
    "v_exp_f32 v94, v94\n\t" \
    "v_exp_f32 v95, v95\n\t" \
    "v_exp_f32 v96, v96\n\t" \
    "v_mfma_f32_32x32x16_bf16 v[76:91], v[140:143], v[36:39], v[44:59]\n\t" \
    "ds_read_b128 v[144:147], v164 offset:7280\n\t" \
    "v_exp_f32 v97, v97\n\t" \
    "v_exp_f32 v98, v98\n\t" \
    "v_exp_f32 v99, v99\n\t" \
    "v_exp_f32 v100, v100\n\t" \
    "v_exp_f32 v101, v101\n\t" \
    "v_smfmac_f32_32x32x32_bf16 v[124:139], v[156:159], v[92:99], %[ix]\n\t" \
    "v_exp_f32 v102, v102\n\t" \
    "v_exp_f32 v103, v103\n\t" \
    "v_exp_f32 v104, v104\n\t" \
    "v_exp_f32 v105, v105\n\t" \
    "v_exp_f32 v106, v106\n\t" \
    "v_exp_f32 v107, v107\n\t" \
    "v_exp_f32 v76, v76\n\t" \
    "v_exp_f32 v77, v77\n\t" \
    "v_smfmac_f32_32x32x32_bf16 v[124:139], v[160:163], v[100:107], %[ix]\n\t" \
    "v_exp_f32 v78, v78\n\t" \
    "v_exp_f32 v79, v79\n\t" \
    "v_exp_f32 v80, v80\n\t" \
    "v_mfma_f32_32x32x16_bf16 v[92:107], v[140:143], v[40:43], v[60:75]\n\t" \
    "s_waitcnt lgkmcnt(0)\n\t" \
    "v_exp_f32 v81, v81\n\t" \
    "v_exp_f32 v82, v82\n\t" \
    "v_exp_f32 v83, v83\n\t" \
    "v_exp_f32 v84, v84\n\t" \
    "v_exp_f32 v85, v85\n\t" \
    "v_smfmac_f32_32x32x32_bf16 v[108:123], v[148:151], v[76:83], %[ix]\n\t" \
    "ds_read_b128 v[156:159], v165 offset:7280\n\t" \
    "v_exp_f32 v86, v86\n\t" \
    "v_exp_f32 v87, v87\n\t" \
    "v_exp_f32 v88, v88\n\t" \
    "v_exp_f32 v89, v89\n\t" \
    "v_exp_f32 v90, v90\n\t" \
    "v_exp_f32 v91, v91\n\t" \
    "v_exp_f32 v92, v92\n\t" \
    "v_exp_f32 v93, v93\n\t" \
    "v_smfmac_f32_32x32x32_bf16 v[108:123], v[152:155], v[84:91], %[ix]\n\t" \
    "ds_read_b128 v[160:163], v166 offset:7280\n\t" \
    "v_exp_f32 v94, v94\n\t" \
    "v_exp_f32 v95, v95\n\t" \
    "v_exp_f32 v96, v96\n\t" \
    "v_mfma_f32_32x32x16_bf16 v[76:91], v[144:147], v[36:39], v[44:59]\n\t" \
    "ds_read_b128 v[140:143], v164 offset:8320\n\t" \
    "v_exp_f32 v97, v97\n\t" \
    "v_exp_f32 v98, v98\n\t" \
    "v_exp_f32 v99, v99\n\t" \
    "v_exp_f32 v100, v100\n\t" \
    "v_exp_f32 v101, v101\n\t" \
    "v_smfmac_f32_32x32x32_bf16 v[124:139], v[148:151], v[92:99], %[ix]\n\t" \
    "v_exp_f32 v102, v102\n\t" \
    "v_exp_f32 v103, v103\n\t" \
    "v_exp_f32 v104, v104\n\t" \
    "v_exp_f32 v105, v105\n\t" \
    "v_exp_f32 v106, v106\n\t" \
    "v_exp_f32 v107, v107\n\t" \
    "v_exp_f32 v76, v76\n\t" \
    "v_exp_f32 v77, v77\n\t" \
    "v_smfmac_f32_32x32x32_bf16 v[124:139], v[152:155], v[100:107], %[ix]\n\t" \
    "v_exp_f32 v78, v78\n\t" \
    "v_exp_f32 v79, v79\n\t" \
    "v_exp_f32 v80, v80\n\t" \
    "v_mfma_f32_32x32x16_bf16 v[92:107], v[144:147], v[40:43], v[60:75]\n\t" \
    "s_waitcnt lgkmcnt(0)\n\t" \
    "v_exp_f32 v81, v81\n\t" \
    "v_exp_f32 v82, v82\n\t" \
    "v_exp_f32 v83, v83\n\t" \
    "v_exp_f32 v84, v84\n\t" \
    "v_exp_f32 v85, v85\n\t" \
    "v_smfmac_f32_32x32x32_bf16 v[108:123], v[156:159], v[76:83], %[ix]\n\t" \
    "ds_read_b128 v[148:151], v165 offset:8320\n\t" \
    "v_exp_f32 v86, v86\n\t" \
    "v_exp_f32 v87, v87\n\t" \
    "v_exp_f32 v88, v88\n\t" \
    "v_exp_f32 v89, v89\n\t" \
    "v_exp_f32 v90, v90\n\t" \
    "v_exp_f32 v91, v91\n\t" \
    "v_exp_f32 v92, v92\n\t" \
    "v_exp_f32 v93, v93\n\t" \
    "v_smfmac_f32_32x32x32_bf16 v[108:123], v[160:163], v[84:91], %[ix]\n\t" \
    "ds_read_b128 v[152:155], v166 offset:8320\n\t" \
    "v_exp_f32 v94, v94\n\t" \
    "v_exp_f32 v95, v95\n\t" \
    "v_exp_f32 v96, v96\n\t" \
    "v_mfma_f32_32x32x16_bf16 v[76:91], v[140:143], v[36:39], v[44:59]\n\t" \
    "ds_read_b128 v[144:147], v164 offset:9360\n\t" \
    "v_exp_f32 v97, v97\n\t" \
    "v_exp_f32 v98, v98\n\t" \
    "v_exp_f32 v99, v99\n\t" \
    "v_exp_f32 v100, v100\n\t" \
    "v_exp_f32 v101, v101\n\t" \
    "v_smfmac_f32_32x32x32_bf16 v[124:139], v[156:159], v[92:99], %[ix]\n\t" \
    "ds_read_b32 %[tv], %[pa]\n\t" \
    "v_exp_f32 v102, v102\n\t" \
    "v_exp_f32 v103, v103\n\t" \
    "v_exp_f32 v104, v104\n\t" \
    "v_exp_f32 v105, v105\n\t" \
    "v_exp_f32 v106, v106\n\t" \
    "v_exp_f32 v107, v107\n\t" \
    "v_exp_f32 v76, v76\n\t" \
    "v_exp_f32 v77, v77\n\t" \
    "v_smfmac_f32_32x32x32_bf16 v[124:139], v[160:163], v[100:107], %[ix]\n\t" \
    "s_add_u32 %[n], %[n], 1\n\t" \
    "v_exp_f32 v78, v78\n\t" \
    "v_exp_f32 v79, v79\n\t" \
    "v_exp_f32 v80, v80\n\t" \
    "v_mfma_f32_32x32x16_bf16 v[92:107], v[140:143], v[40:43], v[60:75]\n\t" \
    "s_waitcnt lgkmcnt(0)\n\t" \
    "v_exp_f32 v81, v81\n\t" \
    "v_exp_f32 v82, v82\n\t" \
    "v_exp_f32 v83, v83\n\t" \
    "v_cmp_gt_u32 vcc, %[n], %[tv]\n\t" \
    "v_add_u32 v164, 8320, v164\n\t" \
    "v_add_u32 v165, 8320, v165\n\t" \
    "v_add_u32 v166, 8320, v166\n\t" \
    "s_cbranch_vccz LT_%=\n\t" \
    "s_cmp_eq_u32 %[n], 8\n\t" \
    "s_cbranch_scc1 LE_%=\n\t" \
    "LW_%=:\n\t" \
    "s_sleep 1\n\t" \
    "ds_read_b32 %[tv], %[pa]\n\t" \
    "s_add_u32 %[sp], %[sp], 1\n\t" \
    "s_lshr_b32 %[q], %[sp], 22\n\t" \
    "s_cbranch_scc1 LT_%=\n\t" \
    "s_waitcnt lgkmcnt(0)\n\t" \
    "v_cmp_gt_u32 vcc, %[n], %[tv]\n\t" \
    "s_nop 4\n\t" \
    "s_cbranch_vccnz LW_%=\n\t" \
    "s_branch LT_%=\n\t" \
    "LE_%=:\n\t" \
    "s_nop 4\n\t"
#define ATT_HEAD_OUT(R) "={v[76:91]}"(R.sa), "={v[92:107]}"(R.sb), "+{v[44:59]}"(R.na), "+{v[60:75]}"(R.nb)
#define ATT_HEAD_IN(R) "{v[36:39]}"(R.qa), "{v[40:43]}"(R.qb), "{v[140:143]}"(R.k0)
#define ATT_OPS(R) "+{v[76:91]}"(R.sa), "+{v[92:107]}"(R.sb), "+{v[108:123]}"(R.oa), "+{v[124:139]}"(R.ob), \
    "+{v[140:143]}"(R.k0), "+{v164}"(R.kp), "+{v165}"(R.vp0), "+{v166}"(R.vp1)
#define ATT_INS(R) "{v[36:39]}"(R.qa), "{v[40:43]}"(R.qb), "{v[44:59]}"(R.na), "{v[60:75]}"(R.nb)
#define ATT_CLOB "v144","v145","v146","v147","v148","v149","v150","v151","v152","v153","v154","v155","v156","v157","v158","v159","v160","v161","v162","v163"
struct AttRegs { f32x16 sa, sb, oa, ob, na, nb; bf16x8 qa, qb, k0; unsigned kp, vp0, vp1; };
#define CNT(i) (lds + L_CNT + (i)*4)

__global__ __launch_bounds__(768, 3) void fused(const uint16_t* __restrict__ x, const uint16_t* __restrict__ wA,
                                                const uint16_t* __restrict__ woA, const uint16_t* __restrict__ wB,
                                                const uint16_t* __restrict__ wphi, const uint16_t* __restrict__ woB,
                                                const uint16_t* __restrict__ gatew, uint16_t* __restrict__ out, char* ws) {
    extern __shared__ __attribute__((aligned(16))) char smem[];
    const lds_ptr lds = (lds_ptr)smem;
    const unsigned lds0 = (unsigned)(uintptr_t)lds;
    int tid = threadIdx.x, lane = tid & 63, r32 = lane & 31, hh = lane >> 5;
    const int wid = __builtin_amdgcn_readfirstlane(tid >> 6);
    const int xc = blockIdx.x & 7, idx = blockIdx.x >> 3;
    const rsrc_t wsr = __builtin_amdgcn_make_buffer_rsrc(ws, 0, WS_END, 0x00020000);
    int q4 = (lane & 15) >> 2, pp = lane & 3, g1 = (lane >> 4) & 1, lr = lane >> 3, lc = lane & 7;
    lds_ptr kp = lds + lane * 16;
#define RELANE() do { asm volatile("" : "+v"(tid)); lane = tid & 63; r32 = lane & 31; hh = lane >> 5; q4 = (lane & 15) >> 2; pp = lane & 3; \
    g1 = (lane >> 4) & 1; lr = lane >> 3; lc = lane & 7; kp = lds + lane * 16; } while (0)
    const f32x16 z16 = {};
    u64 token;

    const int hq = (xc & 3) * 2 + (idx & 1), set = (idx >> 1) & 1, rb = (xc >> 2) * 8 + (idx >> 2);
    const unsigned vx0 = (wid * 16 + lr) * 1024 + ((lc ^ (lr >> 1)) * 16), vx1 = (wid * 16 + 8 + lr) * 1024 + ((lc ^ (4 + (lr >> 1))) * 16);
    const unsigned vw = (wid * 8 + lr) * 3072 + ((lc ^ (((lr >> 1) & 1) << 2)) * 16);
    const uint16_t* xb = x + (size_t)rb * 128 * 512;
    const uint16_t* w0 = wA + (set ? 512 : 0) + (hq ^ 1) * 64;
    const uint16_t* w1 = wB + (set ? 0 : 512) + hq * 64;
    const uint16_t* w2 = (set ? wA : wB) + 1024 + hq * 64;
    const int rot = (((idx >> 2) ^ idx) & 1) * 4;
#define P1_DMA4(va, vb, vc, vd, pa, pb, pc, pd, m0a, m0b, m0c, m0d)                                             \
    asm volatile("s_mov_b32 m0, %8\n\ts_nop 0\n\tglobal_load_lds_dwordx4 %0, %4\n\t"                          \
                 "s_mov_b32 m0, %9\n\ts_nop 0\n\tglobal_load_lds_dwordx4 %1, %5\n\t"                          \
                 "s_mov_b32 m0, %10\n\ts_nop 0\n\tglobal_load_lds_dwordx4 %2, %6\n\t"                         \
                 "s_mov_b32 m0, %11\n\ts_nop 0\n\tglobal_load_lds_dwordx4 %3, %7"                              \
                 :: "v"(va), "v"(vb), "v"(vc), "v"(vd), "s"(pa), "s"(pb), "s"(pc), "s"(pd), "s"(m0a), "s"(m0b), "s"(m0c), "s"(m0d) : "memory")
#define P1_ISSUE(s)                                                                                            \
    do {                                                                                                       \
        const unsigned b_ = lds0 + slot_of(s);                                                                 \
        if ((s) < 8) {                                                                                         \
            const int k_ = ((s) ^ rot) * 64;                                                                   \
            P1_DMA4(vx0, vw, vw, vx1, xb + k_, w0 + k_ * 1536, w2 + k_ * 1536, xb + k_,                        \
                    b_ + wid * 2048, b_ + 16384 + wid * 1024, b_ + 24576 + wid * 1024, b_ + wid * 2048 + 1024); \
        } else {                                                                                               \
            const uint16_t* q_ = w1 + ((((s)-8) * 4) ^ rot) * 64 * 1536;                                       \
            P1_DMA4(vw, vw, vw, vw, q_, q_ + 64 * 1536, q_ + 128 * 1536, q_ + 192 * 1536,                      \
                    b_ + wid * 1024, b_ + 8192 + wid * 1024, b_ + 16384 + wid * 1024, b_ + 24576 + wid * 1024); \
        }                                                                                                      \
    } while (0)
    if (wid < 8) { P1_ISSUE(0); P1_ISSUE(1); }
    const int h = xc * 4 + (idx >> 3), qb = idx & 7;
    const int rb3 = xc * 4 + (idx >> 3), cb = idx & 7;
    gu64* const f2 = (gu64*)(ws + WS_F2) + (hq * 32 + rb * 2 + set) * FS;
    if (tid < 32) *(LAS unsigned*)(lds + L_CNT + tid * 4) = (tid >> 2) == C_PG / 4 + 1 ? 6u : 0u;

#define SWAP23(l) (((l) & ~12) | (((l) & 4) << 1) | (((l) & 8) >> 1))
#define PK_IMG(p, j) ((j) == 7 ? L_PK7 + (p)*2048 : L_PK + (p)*L_V + (j)*2048)
#define PADV(isv) ((isv) ? u32x4{0x3f803f80u, 0x3f803f80u, 0x3f803f80u, 0x3f803f80u} : u32x4{})
    const int wsw = ((q4 >> 1) & 1) << 2;
    if (wid >= 8) {
        const int cw = wid - 8, rh = cw & 1, p = cw >> 1;
        f32x16 a0[2] = {}, a2[2] = {};
        {
            lds_ptr xa[2][4], wa[2];
#pragma unroll
            for (int ks = 0; ks < 4; ++ks) {
                xa[0][ks] = lds + (rh * 64 + r32) * 128 + (((2 * ks + hh) ^ ((r32 >> 1) & 7)) * 16); xa[1][ks] = xa[0][ks] + 2 * SLOT;
                asm volatile("" : "+v"(xa[0][ks]), "+v"(xa[1][ks]));
            }
            wa[0] = lds + 16384 + (8 * hh + q4) * 128 + (((p * 4 + g1 * 2 + (pp >> 1)) ^ wsw) * 16) + (pp & 1) * 8; wa[1] = wa[0] + 2 * SLOT;
            asm volatile("" : "+v"(wa[0]), "+v"(wa[1]));
            bf16x8 fx[3][2], fw[3][2];
#define P1_READ(n)                                                                                       \
    do {                                                                                                 \
        const int so_ = slot_of((n) >> 2), u_ = so_ >= 2 * SLOT, bo_ = so_ - u_ * 2 * SLOT, ks_ = (n)&3, z_ = (n) % 3; \
        fx[z_][0] = ld_b128(xa[u_][ks_] + bo_);                                                          \
        fw[z_][0] = ld_tr2(wa[u_] + bo_ + ks_ * 2048, wa[u_] + bo_ + ks_ * 2048 + 512);                  \
        fx[z_][1] = ld_b128(xa[u_][ks_] + bo_ + 4096);                                                   \
        fw[z_][1] = ld_tr2(wa[u_] + bo_ + 8192 + ks_ * 2048, wa[u_] + bo_ + 8192 + ks_ * 2048 + 512);    \
    } while (0)
            LDS_BAR();
            P1_READ(0); P1_READ(1);
#pragma unroll
            for (int n = 0; n < 32; ++n) {
                if (n == 8) {
                    if (cw == 0) glds16(gatew + lane * 8, lds0 + L_GW);
                    else if (cw < 3) glds16(wphi + hq * 1024 + (cw - 1) * 512 + lane * 8, lds0 + L_WP + (cw - 1) * 1024);
                    else if (lane < 2)
                        asm volatile("s_mov_b32 m0, %1\n\ts_nop 0\n\tglobal_load_lds_dword %0, off sc1" :: "v"(ws + WS_E + lane * 4), "s"(lds0 + L_CNT + 128) : "memory");
                }
                if (n == 30) WAIT_BAR(0); else if ((n & 3) == 2) asm volatile("s_barrier" ::: "memory");
                SBAR();
                if (n < 30) P1_READ(n + 2);
                const int z = n % 3;
                a0[0] = MFMA(fw[z][0], fx[z][0], a0[0]);
                a0[1] = MFMA(fw[z][0], fx[z][1], a0[1]);
                a2[0] = MFMA(fx[z][0], fw[z][1], a2[0]);
                a2[1] = MFMA(fx[z][1], fw[z][1], a2[1]);
                if (n < 30) {
                    __builtin_amdgcn_sched_group_barrier(0x8, 1, 0); __builtin_amdgcn_sched_group_barrier(0x100, 2, 0);
                    __builtin_amdgcn_sched_group_barrier(0x8, 1, 0); __builtin_amdgcn_sched_group_barrier(0x100, 2, 0);
                    __builtin_amdgcn_sched_group_barrier(0x8, 1, 0); __builtin_amdgcn_sched_group_barrier(0x100, 2, 0);
                    __builtin_amdgcn_sched_group_barrier(0x8, 1, 0);
                }
                SBAR();
                if (n == 29) __builtin_amdgcn_s_setprio(3);
            }
        }
        {
            const int tile0 = rb * 4 + rh * 2, h0 = hq * 4 + p * 2;
            if (set == 0) {
                const float c = 0.25f * LOG2E;
#pragma unroll
                for (int i = 0; i < 2; ++i) {
#pragma unroll
                    for (int j = 0; j < 16; ++j) a0[i][j] *= c;
                    const unsigned o = WS_QF + ((h0 ^ 4) * 64 + tile0 + i) * 1024 + lane * 16;
                    st_wt(wsr, o, pack8u<0>(a0[i])); st_wt(wsr, o + 65536, pack8u<8>(a0[i]));
                }
#pragma unroll
                for (int i = 0; i < 2; ++i) {
                    *(LAS u32x4*)(kp + L_TR + (p * 4 + rh * 2 + i) * 2048) = pack8u<0>(a2[i]);
                    *(LAS u32x4*)(kp + L_TR + (p * 4 + rh * 2 + i) * 2048 + 1024) = pack8u<8>(a2[i]);
                }
                lds_arrive(CNT(C_PK), lane);
            } else {
#pragma unroll
                for (int i = 0; i < 2; ++i) {
                    const unsigned o = WS_KF + ((h0 ^ 4) * 64 + tile0 + i) * 1024 + SWAP23(lane) * 16;
                    st_wt(wsr, o, pack8u<0>(a0[i])); st_wt(wsr, o + 65536, pack8u<8>(a0[i]));
                    const unsigned ov = WS_VF + ((h0 + (r32 >> 4)) * 64 + tile0 + i) * 1024 + (hh * 16 + (r32 & 15)) * 16;
                    st_wt(wsr, ov, pack8u<0>(a2[i])); st_wt(wsr, ov + 512, pack8u<8>(a2[i]));
                }
            }
        }
        token = uniform64(*(const LAS u64*)(lds + L_CNT + 128)) + 1;
        signal(CNT(C_F1), 4, (gu64*)(ws + WS_F1) + (hq * 32 + rb * 2 + set) * FS, token, lane);
        const int tile = qb * 8 + cw * 2;
        AttRegs R;
        {
            const int pad = (r32 == 16 || r32 == 28) ? L_V + 1024 : (r32 >= 20 && r32 < 28) ? 4 * VS + 1024 : VS + 1024;
            const int vo = L_V + (hh * 16 + r32) * 16;
            R.kp = lds0 + lane * 16; R.vp0 = lds0 + (r32 < 16 ? vo : pad); R.vp1 = lds0 + (r32 < 16 ? vo + 512 : pad);
            R.oa = z16; R.ob = z16;
            asm volatile("" : "+{v[108:123]}"(R.oa), "+{v[124:139]}"(R.ob), "+{v164}"(R.kp), "+{v165}"(R.vp0), "+{v166}"(R.vp1));
        }
        *(LAS unsigned*)(lds + L_TELL + cw * 256 + lane * 4) = 0u;
        if (cw < 2 && lane < 18) *(LAS u32x4*)(lds + cw * L_V + lane * VS + 1024) = PADV(cw);
        if (cw == 0) {
            lds_wait<2>(CNT(C_F1), 4);
            wait32<2>((gu64*)(ws + WS_F1) + (((h >> 2) ^ hh) * 32) * FS, token, hh ? lane : lane | 1);
            lds_arrive(CNT(C_P1), lane);
        } else {
            lds_wait<1>(CNT(C_P1), 1);
        }
        R.qa = __builtin_bit_cast(bf16x8, ld_dev(wsr, lane * 16, WS_QF + (h * 64 + tile) * 1024));
        R.k0 = __builtin_bit_cast(bf16x8, ld_dev(wsr, lane * 16, WS_KF + h * 65536));
        R.qb = __builtin_bit_cast(bf16x8, ld_dev(wsr, lane * 16 + 1024, WS_QF + (h * 64 + tile) * 1024));
        u32x4 v0[5];
#pragma unroll
        for (int i = 0; i < 5; ++i) v0[i] = ld_dev(wsr, lane * 16 + (i & 3) * 1024, ((cw & 1) ? WS_VF : WS_KF) + h * 65536 + ((cw >> 1) * 5 + (i & 4)) * 1024);
        {
            float ma = -xhalf_max(tile_max(MFMA(R.k0, R.qa, z16))), mb = -xhalf_max(tile_max(MFMA(R.k0, R.qb, z16)));
            asm volatile("" : "=v"(R.na), "=v"(R.nb), "+v"(ma), "+v"(mb));
            R.na[0] = ma; R.nb[0] = mb;
        }
        asm volatile(ATT_HEAD : ATT_HEAD_OUT(R) : ATT_HEAD_IN(R));
#pragma unroll
        for (int i = 0; i < 5; ++i) *(LAS u32x4*)(kp + ((cw & 1) * 64 + (cw >> 1) * 5 + i) * VS) = v0[i];
        lds_arrive(CNT(C_KV), lane);
        RELANE();
        __builtin_amdgcn_s_setprio(1);
        {
            unsigned tv, n, sp, pr, q_;
            asm volatile(ATT_LOOP : ATT_OPS(R), [tv] "=&v"(tv), [n] "=&s"(n), [sp] "=&s"(sp), [p] "=&s"(pr), [q] "=&s"(q_)
                         : ATT_INS(R), [pa] "v"(lds0 + L_CNT + (C_PG + (lane & 7)) * 4), [ix] "v"(0xddddu), [pb] "v"(lds0 + L_TELL + cw * 256 + lane * 4) : ATT_CLOB, "scc", "vcc", "memory");
        }
        RELANE();
        const float ga = *(const LAS float*)(lds + L_G + (cw * 64 + r32) * 4), gb = *(const LAS float*)(lds + L_G + (cw * 64 + 32 + r32) * 4);
        bool bad[2];
#pragma unroll
        for (int u = 0; u < 2; ++u) {
            f32x16 o = u ? R.ob : R.oa;
            const float l = hh ? o[12] : o[8], sc = (u ? gb : ga) * __builtin_amdgcn_rcpf(l);
            float chk = 0.f;
#pragma unroll
            for (int i = 0; i < 8; ++i) { chk += __builtin_fabsf(o[i]); o[i] *= sc; }
            st_wt(wsr, WS_OF + ((tile + u) * 64 + h) * 1024 + lane * 16, pack8u<0>(o));
            bad[u] = !(l <= 1.2089258e24f) || !(l >= 0.25f) || !(chk <= 3.0e38f);
        }
        if (__builtin_expect(__any(bad[0] || bad[1]), 0)) {
#pragma unroll 1
            for (int u = 0; u < 2; ++u) {
                const bf16x8 qf = u ? R.qb : R.qa;
                float mm = -INFINITY;
#pragma unroll 1
                for (int t = 0; t < 64; ++t)
                    mm = __builtin_fmaxf(mm, tile_max(MFMA(__builtin_bit_cast(bf16x8, ld_dev(wsr, lane * 16, WS_KF + (h * 64 + t) * 1024)), qf, z16)));
                mm = xhalf_max(mm);
                f32x16 negm, o;
#pragma unroll
                for (int i = 0; i < 16; ++i) { negm[i] = -mm; o[i] = 0.f; }
#pragma unroll 1
                for (int t = 0; t < 64; ++t) {
                    f32x16 s = MFMA(__builtin_bit_cast(bf16x8, ld_dev(wsr, SWAP23(lane) * 16, WS_KF + (h * 64 + t) * 1024)), qf, negm);
                    u32x4 va = ld_dev(wsr, (hh * 16 + (r32 & 15)) * 16, WS_VF + (h * 64 + t) * 1024), vb = ld_dev(wsr, (hh * 16 + (r32 & 15)) * 16 + 512, WS_VF + (h * 64 + t) * 1024);
#pragma unroll
                    for (int i = 0; i < 4; ++i) { va[i] = r32 < 16 ? va[i] : 0x3f803f80u; vb[i] = r32 < 16 ? vb[i] : 0x3f803f80u; }
#pragma unroll
                    for (int i = 0; i < 16; ++i) s[i] = __builtin_amdgcn_exp2f(s[i]);
                    o = MFMA(__builtin_bit_cast(bf16x8, va), pack8<0>(s), o);
                    o = MFMA(__builtin_bit_cast(bf16x8, vb), pack8<8>(s), o);
                }
                const float sc = (u ? gb : ga) * __builtin_amdgcn_rcpf(hh ? o[12] : o[8]);
#pragma unroll
                for (int i = 0; i < 8; ++i) o[i] *= sc;
                asm volatile("s_waitcnt vmcnt(0)" ::: "memory");
                st_wt(wsr, WS_OF + ((tile + u) * 64 + h) * 1024 + lane * 16, pack8u<0>(o));
            }
        }
        asm volatile("s_waitcnt vmcnt(0)" ::: "memory");
        if (lane == 0) __hip_atomic_store((gu64*)(ws + WS_F3) + ((qb * 4 + cw) * 32 + h) * FS, token, RLX_AGENT);
        return;
    }
    {
        const int p = wid >> 2, rt1 = (wid & 3) ^ (p << 1), h0 = hq * 4 + p * 2;
        f32x16 a1;
        bf16x8 xr[32];
        const bool first = (rt1 >> 1) == set;
        {
            lds_ptr xa[2];
            xa[0] = lds + (rt1 * 32 + r32) * 128 + ((hh ^ (p * 4) ^ ((r32 >> 1) & 7)) * 16); xa[1] = xa[0] + 2 * SLOT;
            asm volatile("" : "+v"(xa[0]), "+v"(xa[1]));
#define P1_TAKE(s)                                                                                              \
    do {                                                                                                        \
        const int so_ = slot_of(s), u_ = so_ >= 2 * SLOT, bo_ = so_ - u_ * 2 * SLOT;                            \
        _Pragma("unroll") for (int k_ = 0; k_ < 4; ++k_) xr[(s)*4 + k_] = ld_b128((lds_ptr)(uintptr_t)((unsigned)(uintptr_t)xa[u_] ^ (k_ * 32)) + bo_); \
    } while (0)
            WAIT_BAR(4); P1_ISSUE(2); P1_TAKE(0);
            WAIT_BAR(4); P1_ISSUE(3); P1_TAKE(1);
            WAIT_BAR(4); P1_ISSUE(4); P1_TAKE(2);
            WAIT_BAR(4); P1_ISSUE(5); P1_TAKE(3);
            WAIT_BAR(4); P1_ISSUE(6); P1_TAKE(4);
            WAIT_BAR(4); P1_ISSUE(7); P1_TAKE(5);
            WAIT_BAR(4); P1_ISSUE(8); P1_TAKE(6);
            WAIT_BAR(4); P1_ISSUE(9); P1_TAKE(7);
            LDS_BAR();
            RELANE();
            asm volatile("s_waitcnt vmcnt(0)" ::: "memory");
            lds_arrive(CNT(C_W1), lane);
            if (first) __builtin_amdgcn_s_setprio(2); else __builtin_amdgcn_s_setprio(1);
            lds_ptr wa = lds + slot_of(8) + (8 * hh + q4) * 128 + (((p * 4 + g1 * 2 + (pp >> 1)) ^ (((q4 >> 1) & 1) << 2)) * 16) + (pp & 1) * 8;
            lds_wait<1>(CNT(C_W1), 8);
            bf16x8 wf[4];
            lds_ptr wb = wa - p * 4096;
            wa += p * 4096;
#define T1_READ(n) wf[(n)&3] = ld_tr2(((n)&2 ? wb : wa) + ((n)&15) * 2048, ((n)&2 ? wb : wa) + ((n)&15) * 2048 + 512)
            T1_READ(0); T1_READ(1); T1_READ(2);
#pragma unroll
            for (int n = 0; n < 32; ++n) {
                if (n == 13) { wa += 2 * SLOT; wb += 2 * SLOT; asm volatile("" : "+v"(wa), "+v"(wb)); }
                SBAR();
                a1 = MFMA(wf[n & 3], xr[n], n == 0 ? z16 : a1);
                SBAR();
                if (n < 29) T1_READ(n + 3);
            }
        }
        RELANE();
        token = uniform64(*(const LAS u64*)(lds + L_CNT + 128)) + 1;
        u32x4 bo[4];
        {
            const unsigned sgn = (r32 & 16) ? 0x80008000u : 0u;
            f32x16 t[2];
#pragma unroll
            for (int e = 0; e < 2; ++e) {
                const lds_ptr wp = lds + L_WP + (p * 2 + e) * 512 + (4 * hh + q4) * 32 + pp * 8;
                u32x4 wu = __builtin_bit_cast(u32x4, ld_tr2(wp, wp + 256));
#pragma unroll
                for (int i = 0; i < 4; ++i) wu[i] ^= sgn;
                t[e] = MFMA(__builtin_bit_cast(bf16x8, wu), e ? pack8<8>(a1) : pack8<0>(a1), z16);
            }
            dual_softmax2(t[0], t[1]);
            if (set) {
                bo[0] = pack8u<0>(t[0]); bo[1] = pack8u<8>(t[0]); bo[2] = pack8u<0>(t[1]); bo[3] = pack8u<8>(t[1]);
                asm volatile("" : "+v"(bo[0]), "+v"(bo[1]), "+v"(bo[2]), "+v"(bo[3]));
            } else {
#pragma unroll
                for (int e = 0; e < 2; ++e)
#pragma unroll
                    for (int g = 0; g < 4; ++g) {
                        u32x2 w2 = {pk2(t[e][4 * g], t[e][4 * g + 1]), pk2(t[e][4 * g + 2], t[e][4 * g + 3])};
                        *(LAS u32x2*)(lds + PK_IMG(p, e * 4 + rt1) + r32 * 64 + (((2 * g + hh) ^ ((r32 >> 1) & 7)) * 8)) = w2;
                    }
                lds_arrive(CNT(C_PK), lane);
            }
            if (first) {
                f32x4 g0 = {}, g1_ = {};
                u32x4 gw[16];
#pragma unroll
                for (int i = 0; i < 16; ++i) gw[i] = *(const LAS u32x4*)(lds + L_GW + (((i >> 1) ^ rot) * 4 + (2 ^ (p * 2)) + (i & 1)) * 32 + hh * 16);
#pragma unroll
                for (int i = 0; i < 16; ++i) {
                    const u32x4 w_ = gw[i], x_ = __builtin_bit_cast(u32x4, xr[(i >> 1) * 4 + 2 + (i & 1)]);
                    g0 = __builtin_amdgcn_mfma_f32_4x4x4bf16_1k(__builtin_bit_cast(s16x4, u32x2{w_[0], w_[1]}), __builtin_bit_cast(s16x4, u32x2{x_[0], x_[1]}), g0, 0, 0, 0);
                    g1_ = __builtin_amdgcn_mfma_f32_4x4x4bf16_1k(__builtin_bit_cast(s16x4, u32x2{w_[2], w_[3]}), __builtin_bit_cast(s16x4, u32x2{x_[2], x_[3]}), g1_, 0, 0, 0);
                }
                *(LAS float*)(lds + L_G + ((p * 2 + hh) * 64 + (rt1 & 1) * 32 + r32) * 4) = g0[0] + g1_[0];
            }
            {
                if (!set && wid < 4) {
                    lds_wait<1>(CNT(C_PK), 12);
                    const int e = wid & 1, pk = wid >> 1;
                    const bool mine = (r32 >> 4) == e;
                    f32x16 kv = z16;
#pragma unroll
                    for (int i = 0; i < 4; ++i)
#pragma unroll
                        for (int ks = 0; ks < 2; ++ks) {
                            const lds_ptr im = lds + PK_IMG(pk, e * 4 + i);
                            const int ra = 16 * ks + 4 * hh + q4, rb_ = ra + 8, sl = g1 * 4 + pp;
                            const bf16x8 af = ld_tr2(im + ra * 64 + ((sl ^ ((ra >> 1) & 7)) * 8), im + rb_ * 64 + ((sl ^ ((rb_ >> 1) & 7)) * 8));
                            u32x4 b = *(const LAS u32x4*)(kp + L_TR + (pk * 4 + i) * 2048 + ks * 1024);
#pragma unroll
                            for (int j = 0; j < 4; ++j) b[j] = mine ? b[j] : 0x3f803f80u;
                            kv = MFMA(af, __builtin_bit_cast(bf16x8, b), kv);
                        }
#pragma unroll
                    for (int g = 0; g < 4; ++g) bo[g] = __builtin_bit_cast(u32x4, f32x4{kv[4 * g], kv[4 * g + 1], kv[4 * g + 2], kv[4 * g + 3]});
                }
                lds_arrive(CNT(C_PD), lane);
            }
        }
        asm volatile("" : "+v"(bo[0]), "+v"(bo[1]), "+v"(bo[2]), "+v"(bo[3]));
        unsigned wor[8], wov = (4 * hh * 512 + r32) * 2;
        asm volatile("" : "+v"(wov));
        __builtin_amdgcn_s_setprio(0);
        const int isv = wid & 1, hf = (wid >> 1) & 1;
        const unsigned so = (isv ? WS_VF : WS_KF) + h * 65536;
        const lds_ptr d1 = kp + (isv * 64 + 2 + hf * 4) * VS, d7 = kp + (isv * 64 + 58 + hf * 3) * VS;
        u32x4 v[3][4];
        unsigned pga = lds0 + L_CNT + (C_PG + (wid ^ 4)) * 4, one = 1;
        asm volatile("" : "+v"(pga), "+v"(one));
#define PG_ARRIVE() asm volatile("s_waitcnt lgkmcnt(0)\n\ts_mov_b64 exec, 1\n\tds_add_u32 %0, %1\n\ts_mov_b64 exec, -1" :: "v"(pga), "v"(one) : "memory")
#define KV_LD(c) _Pragma("unroll") for (int i = 0; i < ((c) == 7 ? 3 : 4); ++i) v[(c) % 3][i] = ld_dev(wsr, lane * 16 + i * 1024, so + ((c) == 7 ? 58 + hf * 3 : 8 * (c) + 2 + hf * 4) * 1024)
#define KV_ST(c) do { _Pragma("unroll") for (int i = 0; i < ((c) == 7 ? 3 : 4); ++i) *(LAS u32x4*)(((c) == 7 ? d7 : d1 + 8 * (c)*VS) + i * VS) = v[(c) % 3][i]; PG_ARRIVE(); } while (0)
        if (wid >= 4) {
            lds_wait<2>(CNT(C_KV), 4);
            KV_LD(1); KV_LD(2);
            KV_LD(3);
            KV_ST(1); KV_LD(4);
            lds_wait<4>(CNT(C_PD), 8);
            if (hf == 0 && lane >= 18) *(LAS u32x4*)(lds + isv * L_V + lane * VS + 1024) = PADV(isv);
        } else {
            const int ks = blockIdx.x >> 2;
            const rsrc_t wor_r = __builtin_amdgcn_make_buffer_rsrc((void*)(ks < 32 ? woA : woB), 0, 512 * 512 * 2, 0x00020000);
            lds_wait<8>(CNT(C_PG), 1);
#pragma unroll
            for (int j = 0; j < 8; ++j)
                wor[j] = __builtin_amdgcn_raw_buffer_load_b16(wor_r, wov + (j & 3) * 1024, (((ks & 31) * 16 + 8 * (j >> 2)) * 512 + ((blockIdx.x & 3) * 4 + wid) * 32) * 2, 0);
        }
        __builtin_amdgcn_s_setprio(2);
        if (set) {
#pragma unroll
            for (int g = 0; g < 4; ++g) st_wt(wsr, WS_PQ + ((h0 + (g >> 1)) * 64 + rb * 4 + rt1) * 2048 + lane * 16 + (g & 1) * 1024, bo[g]);
        } else if (wid < 4) {
            const bool mine = (r32 >> 4) == (wid & 1);
            const int col = mine ? (r32 & 15) : 16;
            if (mine || (r32 & 15) == 0) {
#pragma unroll
                for (int g = 0; g < 4; ++g) st_wt(wsr, WS_KV + ((hq * 4 + wid) * 16 + rb) * KVP + (col * 2 + hh) * 16 + g * 544, bo[g]);
            }
        }
        if (wid < 4)
            st_wt(wsr, WS_WO + (blockIdx.x * 4 + wid) * 1024 + lane * 16,
                  u32x4{wor[0] | (wor[1] << 16), wor[2] | (wor[3] << 16), wor[4] | (wor[5] << 16), wor[6] | (wor[7] << 16)});
        if (wid == 4) {
            const LAS float* gp = (const LAS float*)(lds + L_G) + lane;
            const float gv = __builtin_amdgcn_rcpf(1.f + __builtin_amdgcn_exp2f(-LOG2E * ((gp[0] + gp[64]) + (gp[128] + gp[192]))));
            __builtin_amdgcn_raw_buffer_store_b32(__float_as_uint(gv), wsr, lane * 4, WS_G + (hq * 2048 + rb * 128 + set * 64) * 4, 16);
        }
        if (wid >= 4) KV_ST(2);
        signal(CNT(C_F2), 8, f2, token, lane);
        if (wid >= 4) {
            __builtin_amdgcn_s_setprio(0);
            KV_LD(5); KV_ST(3); KV_LD(6); KV_ST(4); KV_LD(7); KV_ST(5); KV_ST(6); KV_ST(7);
        } else {
            __builtin_amdgcn_s_setprio(0);
            if (wid == 0) {
                lds_wait<8>(CNT(C_F2), 8);
                wait32<12>((gu64*)(ws + WS_F2) + ((h >> 2) * 32) * FS, token, lane);
                lds_arrive(CNT(C_P2), lane);
            } else {
                lds_wait<12>(CNT(C_P2), 1);
            }
            __builtin_amdgcn_s_setprio(2);
            {
                const int tile = qb * 8 + wid * 2;
                const unsigned sk = WS_KV + h * 16 * KVP + wid * 544, vk = lane < 34 ? lane * 16 : 0;
                f32x4 s0 = __builtin_bit_cast(f32x4, ld_dev(wsr, vk, sk));
#pragma unroll
                for (int i = 1; i < 16; ++i) s0 += __builtin_bit_cast(f32x4, ld_dev(wsr, vk, sk + i * KVP));
                u32x4 pq[4];
#pragma unroll
                for (int i = 0; i < 4; ++i) pq[i] = ld_dev(wsr, lane * 16 + i * 1024, WS_PQ + (h * 64 + tile) * 2048);
                const float ga = __uint_as_float(__builtin_amdgcn_raw_buffer_load_b32(wsr, r32 * 4, WS_G + ((h >> 2) * 2048 + tile * 32) * 4, 16));
                const float gb = __uint_as_float(__builtin_amdgcn_raw_buffer_load_b32(wsr, r32 * 4 + 128, WS_G + ((h >> 2) * 2048 + tile * 32) * 4, 16));
                *(LAS u32x2*)(lds + L_TR + lane * 32 + wid * 8) = u32x2{pk2(s0[0], s0[1]), pk2(s0[2], s0[3])};
                *(LAS float*)(lds + L_G + (wid * 64 + lane) * 4) = hh ? gb : ga;
                PG_ARRIVE();
                lds_arrive(CNT(C_S0), lane);
                lds_wait<4>(CNT(C_S0), 4);
                const lds_ptr ks = lds + L_TR + ((r32 < 16 ? r32 : 16) * 2 + hh) * 32;
                const bf16x8 kv0 = ld_b128(ks), kv1 = ld_b128(ks + 16);
                f32x16 nb[2];
                asm volatile("" :: "v"(kv0), "v"(kv1), "v"(pq[0]), "v"(pq[1]), "v"(pq[2]), "v"(pq[3]));
                __builtin_amdgcn_s_setprio(0);
                SBAR();
#pragma unroll
                for (int u = 0; u < 2; ++u) {
                    nb[u] = MFMA(kv0, __builtin_bit_cast(bf16x8, pq[2 * u]), z16);
                    nb[u] = MFMA(kv1, __builtin_bit_cast(bf16x8, pq[2 * u + 1]), nb[u]);
                }
                SBAR();
                __builtin_amdgcn_s_sleep(1);
                __builtin_amdgcn_s_setprio(2);
                SBAR();
#pragma unroll
                for (int u = 0; u < 2; ++u) {
                    const float sc = (1.f - (u ? gb : ga)) * __builtin_amdgcn_rcpf(nb[u][8] + 1e-6f);
#pragma unroll
                    for (int i = 0; i < 8; ++i) nb[u][i] *= sc;
                    st_wt(wsr, WS_OF + ((tile + u) * 64 + 32 + h) * 1024 + lane * 16, pack8u<0>(nb[u]));
                }
                signal(CNT(C_F4), 4, (gu64*)(ws + WS_F4) + (qb * 32 + h) * FS, token, lane);
            }
        }
    }

    __builtin_amdgcn_s_setprio(0);
    RELANE();
    const int j8 = wid, kq = j8 >> 1, rt = j8 & 1;
    f32x16 acc[2];
    u32x4 wf[8][2], of[8];
    const lds_ptr wl = lds + (kq >> 1) * L_V + (kq & 1) * 16384 + lane * 16;
    if (wid == 4) {
        __builtin_amdgcn_s_setprio(2);
#pragma unroll 1
        for (int i = 0; i < 4; ++i) wait32<12>((gu64*)(ws + WS_F2) + (i * 64 + hh * 32) * FS, token, lane);
        lds_arrive(CNT(C_PW), lane);
        __builtin_amdgcn_s_setprio(0);
    } else {
        lds_wait<12>(CNT(C_PW), 1);
    }
    if (wid >= 4) {
        const int kw = wid - 4;
#pragma unroll
        for (int i = 0; i < 8; ++i) {
            wf[i][0] = ld_dev(wsr, lane * 16, WS_WO + ((kw * 8 + i) * 16 + cb * 2) * 1024);
            wf[i][1] = ld_dev(wsr, lane * 16 + 1024, WS_WO + ((kw * 8 + i) * 16 + cb * 2) * 1024);
        }
        const lds_ptr wd = lds + (kw >> 1) * L_V + (kw & 1) * 16384 + lane * 16;
#pragma unroll
        for (int w = 0; w < 4; ++w) lds_wait<12>(lds + L_TELL + w * 256, lds0 + 4 * 8 * VS);
#pragma unroll
        for (int i = 0; i < 8; ++i) { *(LAS u32x4*)(wd + i * 2048) = wf[i][0]; *(LAS u32x4*)(wd + i * 2048 + 1024) = wf[i][1]; }
    }
    {
#pragma unroll
        for (int i = 0; i < 8; ++i) {
            wf[i][0] = ld_dev(wsr, lane * 16, WS_WO + ((32 + kq * 8 + i) * 16 + cb * 2) * 1024);
            wf[i][1] = ld_dev(wsr, lane * 16 + 1024, WS_WO + ((32 + kq * 8 + i) * 16 + cb * 2) * 1024);
        }
        if (wid == 0) { wait32<8>((gu64*)(ws + WS_F4) + ((rb3 >> 2) * 32) * FS, token, lane); lds_arrive(CNT(C_P4), lane); }
        else lds_wait<12>(CNT(C_P4), 1);
#pragma unroll
        for (int i = 0; i < 8; ++i) of[i] = ld_dev(wsr, lane * 16 + (i & 3) * 1024, WS_OF + ((rb3 * 2 + rt) * 64 + 32 + kq * 8 + (i & 4)) * 1024);
        WAIT_BAR(0);
        gu64* fl = (gu64*)(ws + WS_F3) + (rb3 * 32 + kq * 8 + lc) * FS;
        u64 fv = __hip_atomic_load(fl, RLX_AGENT);
        SBAR();
#pragma unroll
        for (int i = 0; i < 8; ++i) {
            acc[0] = MFMA(__builtin_bit_cast(bf16x8, wf[i][0]), __builtin_bit_cast(bf16x8, of[i]), i == 0 ? z16 : acc[0]);
            acc[1] = MFMA(__builtin_bit_cast(bf16x8, wf[i][1]), __builtin_bit_cast(bf16x8, of[i]), i == 0 ? z16 : acc[1]);
        }
        SBAR();
        unsigned todo = 3;
#pragma unroll 1
        for (unsigned spins = 0;; ++spins) {
            unsigned m = (unsigned)__ballot(fv == token);
            if (spins >= (1u << 20)) m = ~0u;
            __builtin_amdgcn_fence(__ATOMIC_ACQUIRE, "wavefront");
            asm volatile("" ::: "memory");
            if ((todo & 1) && (m & 0x0f) == 0x0f) {
#pragma unroll
                for (int i = 0; i < 4; ++i) of[i] = ld_dev(wsr, lane * 16 + i * 1024, WS_OF + ((rb3 * 2 + rt) * 64 + kq * 8) * 1024);
                todo &= 2;
            }
            if ((todo & 2) && (m & 0xf0) == 0xf0) {
#pragma unroll
                for (int i = 0; i < 4; ++i) of[4 + i] = ld_dev(wsr, lane * 16 + i * 1024, WS_OF + ((rb3 * 2 + rt) * 64 + kq * 8 + 4) * 1024);
                todo &= 1;
            }
            if (!todo) break;
            __builtin_amdgcn_s_sleep(2);
            fv = __hip_atomic_load(fl, RLX_AGENT);
        }
#pragma unroll
        for (int i = 0; i < 8; ++i) { wf[i][0] = *(const LAS u32x4*)(wl + i * 2048); wf[i][1] = *(const LAS u32x4*)(wl + i * 2048 + 1024); }
        SBAR();
#pragma unroll
        for (int i = 0; i < 8; ++i) {
            acc[0] = MFMA(__builtin_bit_cast(bf16x8, wf[i][0]), __builtin_bit_cast(bf16x8, of[i]), acc[0]);
            acc[1] = MFMA(__builtin_bit_cast(bf16x8, wf[i][1]), __builtin_bit_cast(bf16x8, of[i]), acc[1]);
        }
    }
    {
        const lds_ptr d = lds + SLOT + (kq >> 1) * L_V + (kq & 1) * 16384 + (rt * 32 + r32) * 256;
#pragma unroll
        for (int ct = 0; ct < 2; ++ct)
#pragma unroll
            for (int q = 0; q < 4; ++q)
                *(LAS f32x4*)(d + (((ct * 8 + q * 2 + hh) ^ (r32 & 15)) * 16)) = f32x4{acc[ct][4 * q], acc[ct][4 * q + 1], acc[ct][4 * q + 2], acc[ct][4 * q + 3]};
    }
    LDS_BAR();
    if (blockIdx.x == 0 && tid == 0) __hip_atomic_store((gu64*)(ws + WS_E), token, RLX_AGENT);
    {
        const int row = j8 * 8 + lr;
        const lds_ptr s0 = lds + row * 256 + (((2 * lc) ^ (row & 15)) * 16), s1 = lds + row * 256 + (((2 * lc + 1) ^ (row & 15)) * 16);
        f32x4 a0 = {}, a1 = {};
#pragma unroll
        for (int k = 0; k < 4; ++k) { const int o = SLOT + (k >> 1) * L_V + (k & 1) * 16384; a0 += *(const LAS f32x4*)(s0 + o); a1 += *(const LAS f32x4*)(s1 + o); }
        u32x4 r = {pk2(a0[0], a0[1]), pk2(a0[2], a0[3]), pk2(a1[0], a1[1]), pk2(a1[2], a1[3])};
        __builtin_amdgcn_raw_buffer_store_b128(r, __builtin_amdgcn_make_buffer_rsrc(out, 0, 2048 * 512 * 2, 0x00020000), ((rb3 * 64 + row) * 512 + cb * 64 + lc * 8) * 2, 0, 16);
    }
}

extern "C" void kernel_launch(void* const* d_in, const int* in_sizes, int n_in, void* d_out, int out_size, void* d_ws,
                              size_t ws_size, hipStream_t stream) {
    (void)hipFuncSetAttribute((const void*)fused, hipFuncAttributeMaxDynamicSharedMemorySize, LDS_BYTES);
    fused<<<256, 768, LDS_BYTES, stream>>>((const uint16_t*)d_in[0], (const uint16_t*)d_in[1], (const uint16_t*)d_in[2],
                                           (const uint16_t*)d_in[4], (const uint16_t*)d_in[5], (const uint16_t*)d_in[6],
                                           (const uint16_t*)d_in[7], (uint16_t*)d_out, (char*)d_ws);
}
```
